```python
import torch

def _softmax(x, axis=-1):
    return torch.softmax(x, dim=axis)

def _gelu(x):
    return torch.nn.functional.gelu(x, approximate='tanh')

def _silu(x):
    return torch.nn.functional.silu(x)

def _sigmoid(x):
    return torch.sigmoid(x)

def _layernorm(x, axis=-1, eps=1e-05):
    m = x.mean(dim=axis, keepdim=True)
    v = x.var(dim=axis, keepdim=True, unbiased=False)
    return (x - m) / torch.sqrt(v + eps)

def _scatter_add(tgt, idx, src, axis=0):
    return tgt.clone().index_add_(axis, idx, src)

def _segment_sum(src, seg, n):
    out = torch.zeros((n,) + src.shape[1:], dtype=src.dtype)
    return out.index_add_(0, seg, src)

def _pad(x, pw):
    flat = tuple((v for pair in reversed(pw) for v in pair))
    return torch.nn.functional.pad(x, flat)
kernel_name = 'standalone__dit_adaln_block__diffusion'

def setup_inputs():

    _w = lambda a: (lambda t: t.to(torch.bfloat16) if t.is_floating_point() else t)(torch.as_tensor(a))
    B, N, D = (2, 1024, 384)
    return {'x': _w((lambda *s: torch.randn(*s, dtype=torch.bfloat16) * 0.05)(B, N, D)), 'c': _w((lambda *s: torch.randn(*s, dtype=torch.bfloat16) * 0.2)(B, D)), 'wqkv': _w((lambda *s: torch.randn(*s, dtype=torch.bfloat16) * 0.05)(D, 3 * D)), 'wo': _w((lambda *s: torch.randn(*s, dtype=torch.bfloat16) * 0.1)(D, D)), 'w_mod': _w((lambda *s: torch.randn(*s, dtype=torch.bfloat16) * 0.2)(D, 6 * D))}

def reference(x, c, wqkv, wo, w_mod):
    B, N, D = x.shape
    mod = (_silu(c) @ w_mod).reshape(B, 6, 1, D)
    shift_a, scale_a, gate_a, shift_m, scale_m, gate_m = [mod[:, i] for i in range(6)]
    h = _layernorm(x, axis=-1) * (1 + scale_a) + shift_a
    qkv = h @ wqkv
    q, k, v = (qkv[..., :D].clone(), qkv[..., D:2 * D].clone(), qkv[..., 2 * D:].clone())
    p = _softmax(torch.einsum('bqd,bkd->bqk', q, k) * D ** (-0.5), axis=-1)
    a = torch.einsum('bqk,bkd->bqd', p, v) @ wo
    return x + gate_a * a
```

```cpp
#include <hip/hip_runtime.h>
#include <hip/hip_bf16.h>

constexpr int B = 2, N = 1024, D = 384;
typedef __hip_bfloat16 bf16;
__device__ __forceinline__ float b2f(bf16 v) { return __bfloat162float(v); }

__global__ void k_mod(const bf16* c, const bf16* wmod, float* mod) {
    int j = blockIdx.x * blockDim.x + threadIdx.x;
    int b = blockIdx.y;
    if (j >= 3 * D) return;
    float acc = 0.f;
    for (int i = 0; i < D; ++i) {
        float cv = b2f(c[b * D + i]);
        float s = cv / (1.f + __expf(-cv));
        acc += s * b2f(wmod[(size_t)i * 6 * D + j]);
    }
    mod[b * 3 * D + j] = acc;
}
__global__ void k_ln(const bf16* x, const float* mod, float* h) {
    int row = blockIdx.x; int b = row / N; int t = threadIdx.x;
    float v[6]; float s = 0.f;
    for (int i = 0; i < 6; ++i) { v[i] = b2f(x[(size_t)row * D + t + 64 * i]); s += v[i]; }
    for (int m = 32; m >= 1; m >>= 1) s += __shfl_xor(s, m);
    float mean = s / D; float q = 0.f;
    for (int i = 0; i < 6; ++i) { float d = v[i] - mean; q += d * d; }
    for (int m = 32; m >= 1; m >>= 1) q += __shfl_xor(q, m);
    float rstd = rsqrtf(q / D + 1e-5f);
    for (int i = 0; i < 6; ++i) { int j = t + 64 * i;
        h[(size_t)row * D + j] = (v[i] - mean) * rstd * (1.f + mod[b * 3 * D + D + j]) + mod[b * 3 * D + j]; }
}
__global__ void k_qkv(const float* h, const bf16* w, float* qkv) {
    int row = blockIdx.y; int j = blockIdx.x * blockDim.x + threadIdx.x;
    if (j >= 3 * D) return;
    float acc = 0.f;
    for (int i = 0; i < D; ++i) acc += h[(size_t)row * D + i] * b2f(w[(size_t)i * 3 * D + j]);
    qkv[(size_t)row * 3 * D + j] = acc;
}
__global__ void k_attn(const float* qkv, float* o) {
    __shared__ float s[N]; __shared__ float qs[D]; __shared__ float red[8];
    int row = blockIdx.x; int b = row / N; int t = threadIdx.x;
    for (int i = t; i < D; i += 256) qs[i] = qkv[(size_t)row * 3 * D + i];
    __syncthreads();
    float mx = -1e30f;
    for (int k = t; k < N; k += 256) {
        const float* kr = qkv + (size_t)(b * N + k) * 3 * D + D;
        float acc = 0.f;
        for (int i = 0; i < D; ++i) acc += qs[i] * kr[i];
        acc *= 0.05103103630798288f;
        s[k] = acc; mx = fmaxf(mx, acc);
    }
    for (int m = 32; m >= 1; m >>= 1) mx = fmaxf(mx, __shfl_xor(mx, m));
    if ((t & 63) == 0) red[t >> 6] = mx;
    __syncthreads();
    mx = fmaxf(fmaxf(red[0], red[1]), fmaxf(red[2], red[3]));
    float sum = 0.f;
    for (int k = t; k < N; k += 256) { float p = __expf(s[k] - mx); s[k] = p; sum += p; }
    for (int m = 32; m >= 1; m >>= 1) sum += __shfl_xor(sum, m);
    if ((t & 63) == 0) red[4 + (t >> 6)] = sum;
    __syncthreads();
    sum = red[4] + red[5] + red[6] + red[7];
    for (int j = t; j < D; j += 256) {
        float acc = 0.f;
        for (int k = 0; k < N; ++k) acc += s[k] * qkv[(size_t)(b * N + k) * 3 * D + 2 * D + j];
        o[(size_t)row * D + j] = acc / sum;
    }
}
__global__ void k_out(const float* o, const bf16* wo, const bf16* x, const float* mod, bf16* out) {
    int row = blockIdx.y; int b = row / N; int j = blockIdx.x * blockDim.x + threadIdx.x;
    if (j >= D) return;
    float acc = 0.f;
    for (int i = 0; i < D; ++i) acc += o[(size_t)row * D + i] * b2f(wo[(size_t)i * D + j]);
    out[(size_t)row * D + j] = __float2bfloat16(b2f(x[(size_t)row * D + j]) + mod[b * 3 * D + 2 * D + j] * acc);
}

extern "C" void kernel_launch(void* const* d_in, const int* in_sizes, int n_in,
                              void* d_out, int out_size, void* d_ws, size_t ws_size,
                              hipStream_t stream) {
    const bf16* x = (const bf16*)d_in[0];
    const bf16* c = (const bf16*)d_in[1];
    const bf16* wqkv = (const bf16*)d_in[2];
    const bf16* wo = (const bf16*)d_in[3];
    const bf16* wmod = (const bf16*)d_in[4];
    bf16* out = (bf16*)d_out;
    float* mod = (float*)d_ws;
    float* h = mod + 4096;
    float* qkv = h + (size_t)B * N * D;
    float* o = qkv + (size_t)B * N * 3 * D;
    k_mod<<<dim3(9, 2), 128, 0, stream>>>(c, wmod, mod);
    k_ln<<<B * N, 64, 0, stream>>>(x, mod, h);
    k_qkv<<<dim3(9, B * N), 128, 0, stream>>>(h, wqkv, qkv);
    k_attn<<<B * N, 256, 0, stream>>>(qkv, o);
    k_out<<<dim3(3, B * N), 128, 0, stream>>>(o, wo, x, mod, out);
}
```

```cpp
#include <hip/hip_runtime.h>
#include <hip/hip_bf16.h>

typedef unsigned short u16;
typedef unsigned long long u64;
typedef __bf16 bf16x2 __attribute__((ext_vector_type(2)));
typedef __bf16 bf16x8 __attribute__((ext_vector_type(8)));
typedef short s16x4 __attribute__((ext_vector_type(4)));
typedef float f32x2 __attribute__((ext_vector_type(2)));
typedef float f32x4 __attribute__((ext_vector_type(4)));
typedef float f32x16 __attribute__((ext_vector_type(16)));
typedef unsigned u32x2 __attribute__((ext_vector_type(2)));
typedef unsigned u32x4 __attribute__((ext_vector_type(4)));
#define GAS __attribute__((address_space(1)))
#define LAS __attribute__((address_space(3)))
#define DEV __device__ __forceinline__
#define RLX_AGENT __ATOMIC_RELAXED, __HIP_MEMORY_SCOPE_AGENT
#define SB() __builtin_amdgcn_sched_barrier(0)

constexpr int D = 384;
constexpr unsigned WS_EPOCH = 0;
constexpr unsigned WS_MODG = 256;
constexpr unsigned WS_IN0 = WS_MODG + 8 * 4608;
constexpr unsigned WS_IN1Q = WS_IN0 + 256 * 512;
constexpr unsigned WS_IN1V = WS_IN1Q + 256 * 128;
constexpr unsigned WS_IN2 = WS_IN1V + 256 * 256;
constexpr unsigned WS_MF = WS_IN2 + 256 * 128;
constexpr unsigned WS_QF = WS_MF + 2 * 294912;
constexpr unsigned WS_VF = WS_QF + 1572864;
constexpr unsigned WS_OP = WS_VF + 1572864;
constexpr unsigned WS_ST = WS_OP + 256 * 49152;
constexpr unsigned WS_END = WS_ST + 256 * 2048;
constexpr unsigned WS_STAMP = WS_END;
constexpr int L_XK = 0;
constexpr int L_H = 98304;
constexpr int L_MOD = 147456;
constexpr int L_ML = 152064;
constexpr int L_TOTAL = 152576;
constexpr int SPIN_CAP = 1 << 16;
constexpr int NWAVES = 8;

#define STAMP(i) do {} while (0)
#define NST 0

DEV void wg_barrier() { asm volatile("s_waitcnt lgkmcnt(0)\n\ts_barrier" ::: "memory"); }
DEV void drain() { asm volatile("s_waitcnt vmcnt(0)" ::: "memory"); }
DEV unsigned pack2(float a, float b) { f32x2 f = {a, b}; return __builtin_bit_cast(unsigned, __builtin_convertvector(f, bf16x2)); }
DEV unsigned pack2(f32x2 f) { return __builtin_bit_cast(unsigned, __builtin_convertvector(f, bf16x2)); }
DEV float blo(unsigned u) { return __uint_as_float(u << 16); }
DEV float bhi(unsigned u) { return __uint_as_float(u & 0xffff0000u); }
DEV f32x2 unpack2(unsigned u) { return (f32x2){blo(u), bhi(u)}; }
DEV f32x4 mfma16(u32x4 a, u32x4 b, f32x4 c) { return __builtin_amdgcn_mfma_f32_16x16x32_bf16(__builtin_bit_cast(bf16x8, a), __builtin_bit_cast(bf16x8, b), c, 0, 0, 0); }
DEV f32x16 mfma32(u32x4 a, u32x4 b, f32x16 c) { return __builtin_amdgcn_mfma_f32_32x32x16_bf16(__builtin_bit_cast(bf16x8, a), __builtin_bit_cast(bf16x8, b), c, 0, 0, 0); }
DEV float rows_sum(float v) {
    auto a = __builtin_amdgcn_permlane16_swap(__float_as_uint(v), __float_as_uint(v), false, false);
    v = __uint_as_float(a[0]) + __uint_as_float(a[1]);
    auto b = __builtin_amdgcn_permlane32_swap(__float_as_uint(v), __float_as_uint(v), false, false);
    return __uint_as_float(b[0]) + __uint_as_float(b[1]);
}
DEV float rows_max(float v) {
    auto a = __builtin_amdgcn_permlane16_swap(__float_as_uint(v), __float_as_uint(v), false, false);
    v = fmaxf(__uint_as_float(a[0]), __uint_as_float(a[1]));
    auto b = __builtin_amdgcn_permlane32_swap(__float_as_uint(v), __float_as_uint(v), false, false);
    return fmaxf(__uint_as_float(b[0]), __uint_as_float(b[1]));
}
DEV f32x2 ln_stats(const u32x4 (&f)[12], int r, int g) {
    const u32x4 ones = {0x3f803f80u, 0x3f803f80u, 0x3f803f80u, 0x3f803f80u};
    f32x4 sm = {}, gr = {};
#pragma unroll
    for (int s = 0; s < 12; ++s) { sm = mfma16(ones, f[s], sm); gr = mfma16(f[s], f[s], gr); }
    float d = (r & 2) ? ((r & 1) ? gr[3] : gr[2]) : ((r & 1) ? gr[1] : gr[0]);
    d = rows_sum(g == (r >> 2) ? d : 0.f);
    const float mu = sm[0] * (1.f / D);
    const float rstd = __builtin_amdgcn_rsqf(fmaxf(d * (1.f / D) - mu * mu, 0.f) + 1e-5f);
    return (f32x2){rstd, -mu * rstd};
}
DEV void poll_flags(char* ws, unsigned off, unsigned tag) {
    GAS unsigned* p = (GAS unsigned*)(ws + off);
#pragma unroll 1
    for (int spin = 0; spin < SPIN_CAP; ++spin) {
        unsigned v = __hip_atomic_load(p, RLX_AGENT);
        if (__all(v == tag)) break;
        __builtin_amdgcn_s_sleep(1);
    }
    asm volatile("" ::: "memory");
}
DEV void put_flag(char* ws, unsigned off, unsigned tag) { __hip_atomic_store((GAS unsigned*)(ws + off), tag, RLX_AGENT); }
#define U4(p, off) (*(LAS u32x4*)((p) + (off)))
#define F4(p, off) (*(LAS f32x4*)((p) + (off)))
#define LD16(voff, soff) __builtin_amdgcn_raw_buffer_load_b128(rs, (voff), (soff), 16)
#define ST16(v, voff, soff) __builtin_amdgcn_raw_buffer_store_b128((v), rs, (voff), (soff), 16)
#define ST8(v, voff, soff) __builtin_amdgcn_raw_buffer_store_b64((v), rs, (voff), (soff), 16)
DEV LAS char* opaque(LAS char* p) { asm volatile("" : "+v"(p)); return p; }

extern "C" __global__ __launch_bounds__(512, 2) void dit_fused(const u16* __restrict__ x, const u16* __restrict__ cvec, const u16* __restrict__ wqkv,
                                                               const u16* __restrict__ wo, const u16* __restrict__ wmod, u16* __restrict__ out, char* ws) {
    __shared__ __attribute__((aligned(16))) char smem[L_TOTAL];
    LAS char* lds = (LAS char*)smem;
    const int tid = threadIdx.x, lane = tid & 63;
    const int wave = __builtin_amdgcn_readfirstlane(tid >> 6);
    const int bid = blockIdx.x, xg = bid & 7, idx = bid >> 3;
    unsigned pf0;
    {
        u64 pc; unsigned len;
        asm volatile("s_getpc_b64 %0\n.Lpf_here:\n\ts_mov_b32 %1, .Lpf_end-.Lpf_here" : "=s"(pc), "=s"(len));
        unsigned o0 = min((unsigned)(((idx & 3) * 8 + wave) * 8 + (lane & 7)) * 128u, len);
        asm volatile("global_load_dword %0, %1, %2" : "=&v"(pf0) : "v"(o0), "s"(pc) : "memory");
    }
    asm volatile("" :: "s"(x), "s"(cvec), "s"(wqkv), "s"(wo), "s"(wmod), "s"(out), "s"(ws));
    u64 ep;
    asm volatile("s_load_dwordx2 %0, %1, 0x0" : "=s"(ep) : "s"(ws) : "memory");
    STAMP(0);
    const __amdgpu_buffer_rsrc_t rs = __builtin_amdgcn_make_buffer_rsrc(ws, 0, WS_END, 0x00020000);

    const int b = xg >> 2;
    const int qbg = b * 16 + (xg & 3) * 4 + (idx >> 3);
    const int j = idx & 7;
    const int cid = qbg * 8 + j;
    const int r = lane & 15, g = lane >> 4;
    const int role = idx < 12 ? 0 : idx < 24 ? 1 : idx < 27 ? 2 : 3;
    const int w4 = wave & 3;
    LAS char* const pH = opaque(lds + L_H + lane * 16);

    u32x4 xk[12];
    const u16* kp = x + (b * 1024 + 128 * j + 16 * wave + r) * D + 8 * g;
    unsigned tag0, tag1, tag;
#define GET_TAGS() do { u64 e_; asm volatile("s_waitcnt lgkmcnt(0)\n\ts_mov_b64 %0, %1" : "=s"(e_) : "s"(ep)); \
        tag0 = (unsigned)e_ + 1u; tag1 = (unsigned)(e_ >> 32) + 1u; tag = b ? tag1 : tag0; } while (0)

    if (wave < 4) {
        u32x4 xq[12];
        const u16* xp = x + (64 * qbg + 16 * wave + r) * D + 8 * g;
#pragma unroll
        for (int s = 0; s < 12; ++s) xq[s] = *(const u32x4*)(xp + 32 * s);
        SB();
        GET_TAGS();
        STAMP(1);
        if (role < 3) wg_barrier();
#pragma unroll
        for (int s = 0; s < 12; ++s) xk[s] = *(const u32x4*)(kp + 32 * s);
        SB();
        const f32x2 st = ln_stats(xq, r, g);
        asm volatile("" :: "v"(pf0));
        f32x2 n[48];
#pragma unroll
        for (int s = 0; s < 12; ++s)
#pragma unroll
            for (int e = 0; e < 4; ++e) n[4 * s + e] = unpack2(xq[s][e]) * st[0] + st[1];
#pragma unroll
        for (int i = 0; i < 48; ++i) asm volatile("" : "+v"(n[i]));
        STAMP(3);
        wg_barrier();
        LAS char* pm = opaque(lds + L_MOD + g * 32);
#pragma unroll
        for (int s = 0; s < 12; ++s) {
            const f32x4 t0 = F4(pm, s * 128), t1 = F4(pm, s * 128 + 16), c0 = F4(pm, s * 128 + 1536), c1 = F4(pm, s * 128 + 1536 + 16);
            u32x4 o;
            o[0] = pack2(n[4 * s + 0] * c0.xy + t0.xy);
            o[1] = pack2(n[4 * s + 1] * c0.zw + t0.zw);
            o[2] = pack2(n[4 * s + 2] * c1.xy + t1.xy);
            o[3] = pack2(n[4 * s + 3] * c1.zw + t1.zw);
            U4(pH, (wave * 12 + s) * 1024) = o;
        }
        STAMP(4);
    } else {
        const int ti = idx - (role == 1 ? 12 : 0);
        const int s0 = 3 * (xg >> 1) + (ti >> 2), jb = 4 * (xg & 1) + (ti & 3), slice = xg * 3 + (idx - 24);
        if (role < 3) {
            u32x4 af[2][3], bf[9];
            const int k0 = 96 * w4 + 8 * g;
            const u16* ap; int rtstep;
            if (role == 2) { ap = cvec + (r & 1) * D + k0; rtstep = 0; }
            else { ap = wqkv + (32 * s0 + 8 * (r >> 2) + (r & 3)) * 1152 + (role ? 768 : 0) + k0; rtstep = 4 * 1152; }
            if (role == 0) {
                const u16* bp = wqkv + (48 * jb + r) * 1152 + 384 + k0;
#pragma unroll
                for (int ct = 0; ct < 3; ++ct)
#pragma unroll
                    for (int ks = 0; ks < 3; ++ks) bf[ct * 3 + ks] = *(const u32x4*)(bp + ct * 16 * 1152 + 32 * ks);
            } else {
                const int ldb = role == 1 ? D : 6 * D;
                const u16* bp = (role == 1 ? wo + 48 * jb : wmod + 48 * slice) + 96 * w4 * ldb;
#pragma unroll
                for (int i = 0; i < 3; ++i) {
                    const int c = 64 * i + lane, row = c / 6, ch = c - row * 6;
#pragma unroll
                    for (int ks = 0; ks < 3; ++ks) bf[ks * 3 + i] = *(const u32x4*)(bp + (32 * ks + row) * ldb + 8 * ch);
                }
            }
#pragma unroll
            for (int rt = 0; rt < 2; ++rt)
#pragma unroll
                for (int ks = 0; ks < 3; ++ks) af[rt][ks] = *(const u32x4*)(ap + rt * rtstep + 32 * ks);
            SB();
            STAMP(1);
            if (role != 0) {
#pragma unroll
                for (int i = 0; i < 9; ++i) U4(pH, w4 * 9216 + i * 1024) = bf[i];
                LAS char* tb = opaque(lds + L_H + w4 * 9216 + (8 * g + ((lane >> 2) & 3)) * 96 + (lane & 3) * 8);
#pragma unroll
                for (int ks = 0; ks < 3; ++ks)
#pragma unroll
                    for (int ct = 0; ct < 3; ++ct) {
                        s16x4 lo = __builtin_amdgcn_ds_read_tr16_b64_v4i16((LAS s16x4*)(tb + ks * 3072 + ct * 32));
                        s16x4 hi = __builtin_amdgcn_ds_read_tr16_b64_v4i16((LAS s16x4*)(tb + ks * 3072 + ct * 32 + 384));
                        u32x2 l2 = __builtin_bit_cast(u32x2, lo), h2 = __builtin_bit_cast(u32x2, hi);
                        bf[ct * 3 + ks] = (u32x4){l2[0], l2[1], h2[0], h2[1]};
                    }
                SB();
                if (role == 2) {
#pragma unroll
                    for (int ks = 0; ks < 3; ++ks) {
#pragma unroll
                        for (int e = 0; e < 4; ++e) {
                            float a = blo(af[0][ks][e]), c = bhi(af[0][ks][e]);
                            a = a * __builtin_amdgcn_rcpf(1.f + __builtin_amdgcn_exp2f(-1.4426950408889634f * a));
                            c = c * __builtin_amdgcn_rcpf(1.f + __builtin_amdgcn_exp2f(-1.4426950408889634f * c));
                            af[0][ks][e] = pack2(a, c);
                        }
                    }
                }
            }
            f32x4 acc[2][3] = {};
#pragma unroll
            for (int ks = 0; ks < 3; ++ks)
#pragma unroll
                for (int rt = 0; rt < 2; ++rt)
#pragma unroll
                    for (int ct = 0; ct < 3; ++ct) acc[rt][ct] = mfma16(af[rt][ks], bf[ct * 3 + ks], acc[rt][ct]);
#pragma unroll
            for (int ct = 0; ct < 3; ++ct)
#pragma unroll
                for (int rt = 0; rt < 2; ++rt) F4(pH, w4 * 9216 + (ct * 2 + rt) * 1024) = acc[rt][ct];
            STAMP(2);
            wg_barrier();
        }
        GET_TAGS();
        if (wave < 7) {
            if (role < 3) {
                f32x4 lo = F4(pH, w4 * 2048), hi = F4(pH, w4 * 2048 + 1024);
#pragma unroll
                for (int w = 1; w < 4; ++w) { lo += F4(pH, w * 9216 + w4 * 2048); hi += F4(pH, w * 9216 + w4 * 2048 + 1024); }
                if (role < 2) {
                    u32x4 o = {pack2(lo[0], lo[1]), pack2(lo[2], lo[3]), pack2(hi[0], hi[1]), pack2(hi[2], hi[3])};
                    ST16(o, lane * 16, WS_MF + role * 294912 + ((3 * jb + w4) * 12 + s0) * 1024);
                } else {
                    const unsigned u = pack2(lo[0], lo[1]);
                    const int pr = lane & 7;
                    const unsigned u0 = __shfl(u, 2 * pr), u1 = __shfl(u, 2 * pr + 1);
                    const unsigned pay = lane < 32 ? ((u0 & 0xffffu) | (u1 << 16)) : ((u0 >> 16) | (u1 & 0xffff0000u));
                    __hip_atomic_store((GAS u64*)(ws + WS_MODG + (lane >> 3) * 4608 + (24 * slice + 8 * w4 + pr) * 8), ((u64)(lane < 32 ? tag0 : tag1) << 32) | pay, RLX_AGENT);
                }
            }
            SB();
#pragma unroll
            for (int s = 0; s < 12; ++s) xk[s] = *(const u32x4*)(kp + 32 * s);
            SB();
            if (role < 2) {
                asm volatile("s_waitcnt vmcnt(12)" ::: "memory");
                if (lane < 32) put_flag(ws, WS_IN0 + (lane * 8 + jb) * 512 + role * 256 + (w4 * 12 + s0) * 4, lane < 16 ? tag0 : tag1);
            }
            STAMP(3);
            wg_barrier();
        } else {
            GAS u64* gp = (GAS u64*)(ws + WS_MODG + xg * 4608) + lane;
            u64 gv[9];
#pragma unroll 1
            for (int spin = 0; spin < SPIN_CAP; ++spin) {
                bool ok = true;
#pragma unroll
                for (int i = 0; i < 9; ++i) gv[i] = __hip_atomic_load(gp + 64 * i, RLX_AGENT);
#pragma unroll
                for (int i = 0; i < 9; ++i) ok &= (unsigned)(gv[i] >> 32) == tag;
                if (__all(ok)) break;
                __builtin_amdgcn_s_sleep(1);
            }
#pragma unroll
            for (int i = 0; i < 9; ++i) {
                f32x2 v = unpack2((unsigned)gv[i]);
                if (i >= 3 && i < 6) v += 1.f;
                *(LAS f32x2*)(lds + L_MOD + i * 512 + lane * 8) = v;
            }
            STAMP(3);
            wg_barrier();
            poll_flags(ws, WS_IN0 + cid * 512 + min(lane, 35) * 4, tag);
            STAMP(4);
        }
        asm volatile("" :: "v"(pf0));
    }
    wg_barrier();
    STAMP(5);

    const int ct = 3 * j + w4;
    const int vw = ct / 6, vc = ct - vw * 6;
#define STAGE1_GEMM(MBASE, MFMA_EXPR)                                                                  \
        u32x4 mf[12], hf[12][4];                                                                       \
        _Pragma("unroll") for (int s = 0; s < 12; ++s) mf[s] = LD16(lane * 16, (MBASE) + s * 1024);    \
        SB();                                                                                          \
        _Pragma("unroll") for (int s = 0; s < 5; ++s)                                                  \
            _Pragma("unroll") for (int tt = 0; tt < 4; ++tt) hf[s][tt] = U4(pH, (tt * 12 + s) * 1024); \
        SB();                                                                                          \
        f32x4 ac[4] = {};                                                                              \
        _Pragma("unroll") for (int s = 0; s < 12; ++s) {                                               \
            _Pragma("unroll") for (int tt = 0; tt < 4; ++tt) {                                         \
                ac[tt] = MFMA_EXPR;                                                                    \
                if (s + 5 < 12) hf[s + 5][tt] = U4(pH, (tt * 12 + s + 5) * 1024);                      \
                SB();                                                                                  \
            }                                                                                          \
        }
    if (wave < 3) {
        __builtin_amdgcn_s_setprio(2);
        const f32x4 qs = F4(lds, L_MOD + 1536 + (16 * ct + 4 * g) * 4) * (1.4426950408889634f * 0.05103103630798288f);
        STAGE1_GEMM(WS_MF + ct * 12288, mfma16(mf[s], hf[s][tt], ac[tt]))
        const unsigned vo = ((2 * (ct & 1) + (g >> 1)) * 16 + r) * 16 + (g & 1) * 8;
#pragma unroll
        for (int tt = 0; tt < 4; ++tt) {
            const f32x4 v = ac[tt] * qs;
            u32x2 o = {pack2(v.xy), pack2(v.zw)};
            ST8(o, vo, WS_QF + ((4 * qbg + tt) * 12 + (ct >> 1)) * 1024);
        }
        __builtin_amdgcn_s_setprio(0);
        SB();
        STAMP(6);
    } else if (w4 < 3) {
        poll_flags(ws, WS_IN0 + cid * 512 + 256 + w4 * 48 + min(lane, 11) * 4, tag);
        STAGE1_GEMM(WS_MF + 294912 + ct * 12288, mfma16(hf[s][tt], mf[s], ac[tt]))
        const unsigned vo = (g >> 1) * 1024 + ((g & 1) * 32 + 16 * (ct & 1) + r) * 16;
#pragma unroll
        for (int tp = 0; tp < 2; ++tp) {
            u32x4 o = {pack2(ac[2 * tp].xy), pack2(ac[2 * tp].zw), pack2(ac[2 * tp + 1].xy), pack2(ac[2 * tp + 1].zw)};
            ST16(o, vo, WS_VF + ((((qbg >> 1) * 4 + vw) * 3 + (vc >> 1)) * 8 + 4 * (qbg & 1) + 2 * tp) * 1024);
        }
        SB();
        STAMP(6);
    } else if (wave == 7) {
#pragma unroll
        for (int s = 0; s < 12; ++s) xk[s] = *(const u32x4*)(kp + 32 * s);
        SB();
    }
    {
        const f32x2 st = ln_stats(xk, r, g);
        LAS char* pk = opaque(lds + L_XK + wave * 12288 + lane * 16);
#pragma unroll
        for (int s = 0; s < 12; ++s) {
            u32x4 o;
#pragma unroll
            for (int e = 0; e < 4; ++e) o[e] = pack2(unpack2(xk[s][e]) * st[0] + st[1]);
            U4(pk, s * 1024) = o;
        }
    }
    SB();
    STAMP(7);
    if (w4 < 3) {
        drain();
        if (wave < 3) { if (lane < 8) put_flag(ws, WS_IN1Q + (qbg * 8 + lane) * 128 + ct * 4, tag); }
        else { if (lane < 16) put_flag(ws, WS_IN1V + ((b * 16 + lane) * 8 + ((qbg >> 1) & 7)) * 256 + vw * 64 + ((qbg & 1) * 6 + vc) * 4, tag); }
    }
    STAMP(8);
    wg_barrier();

    const int unit = wave;
    const int tok = 64 * qbg + 32 * (j & 1) + (lane & 31), d0 = 96 * (j >> 1) + 16 * unit + 4 * (lane >> 5);
    u32x2 xr[2];
    if (wave < 4) {
        LAS char* pk0 = opaque(lds + L_XK + lane * 16);
        LAS char* pk1 = opaque(lds + L_XK + 49152 + lane * 16);
        u32x4 kf[12][8], qf[12];
#define KF(s, t) ((t) < 4 ? U4(pk0, ((t) * 12 + (s)) * 1024) : U4(pk1, (((t) - 4) * 12 + (s)) * 1024))
#pragma unroll
        for (int s = 0; s < 4; ++s)
#pragma unroll
            for (int t = 0; t < 8; ++t) kf[s][t] = KF(s, t);
        SB();
        poll_flags(ws, WS_IN1Q + cid * 128 + min(lane, 23) * 4, tag);
        __builtin_amdgcn_s_setprio(2);
        STAMP(9);
#pragma unroll
        for (int s = 0; s < 12; ++s) qf[s] = LD16(lane * 16, WS_QF + ((4 * qbg + wave) * 12 + s) * 1024);
        SB();
        f32x4 sa[8] = {};
#pragma unroll
        for (int s = 0; s < 12; ++s) {
#pragma unroll
            for (int t = 0; t < 8; ++t) {
                sa[t] = mfma16(kf[s][t], qf[s], sa[t]);
                if (s + 4 < 12) kf[s + 4][t] = KF(s + 4, t);
                SB();
            }
        }
        STAMP(11);
        float mx = fmaxf(fmaxf(sa[0][0], sa[0][1]), fmaxf(sa[0][2], sa[0][3]));
#pragma unroll
        for (int t = 1; t < 8; ++t) mx = fmaxf(fmaxf(fmaxf(mx, sa[t][0]), sa[t][1]), fmaxf(sa[t][2], sa[t][3]));
        mx = rows_max(mx);
        f32x4 ls = {};
#pragma unroll
        for (int t = 0; t < 8; ++t) {
            sa[t] -= mx;
#pragma unroll
            for (int e = 0; e < 4; ++e) sa[t][e] = __builtin_amdgcn_exp2f(sa[t][e]);
            ls += sa[t];
        }
        LAS char* pp = lds + L_H + ((wave >> 1) * 8 + (g >> 1)) * 1024 + ((g & 1) * 32 + (wave & 1) * 16 + r) * 16;
#pragma unroll
        for (int tp = 0; tp < 4; ++tp) {
            u32x4 o = {pack2(sa[2 * tp].xy), pack2(sa[2 * tp].zw), pack2(sa[2 * tp + 1].xy), pack2(sa[2 * tp + 1].zw)};
            U4(pp, tp * 2048) = o;
        }
        const float l = rows_sum((ls[0] + ls[1]) + (ls[2] + ls[3]));
        if (g == 0) *(LAS f32x2*)(lds + L_ML + (16 * wave + r) * 8) = (f32x2){mx, l};
        __builtin_amdgcn_s_setprio(0);
        STAMP(12);
        wg_barrier();
        xr[0] = *(const u32x2*)(x + tok * D + d0); xr[1] = *(const u32x2*)(x + tok * D + d0 + 8);
    } else {
        u32x4 vf[24], pf[16];
        if (wave < 6) { xr[0] = *(const u32x2*)(x + tok * D + d0); xr[1] = *(const u32x2*)(x + tok * D + d0 + 8); }
        poll_flags(ws, WS_IN1V + cid * 256 + w4 * 64 + min(lane, 11) * 4, tag);
        STAMP(10);
#pragma unroll
        for (int i = 0; i < 24; ++i) vf[i] = LD16(lane * 16, WS_VF + (((b * 8 + j) * 4 + w4) * 24 + i) * 1024);
        SB();
        wg_barrier();
        __builtin_amdgcn_s_setprio(2);
        STAMP(12);
#pragma unroll
        for (int i = 0; i < 16; ++i) pf[i] = U4(pH, i * 1024);
        const u32x2 ml = *(LAS u32x2*)(lds + L_ML + lane * 8);
        SB();
        ST8(ml, (lane >> 5) * 2048 + (lane & 31) * 8, WS_ST + (qbg * 8 + 2 * w4) * 2048 + j * 256);
        f32x16 oa[6];
#pragma unroll
        for (int u = 0; u < 7; ++u) {
            if (u < 6) {
                oa[u] = (f32x16){};
#pragma unroll
                for (int ks = 0; ks < 8; ++ks) oa[u] = mfma32(vf[(u >> 1) * 8 + ks], pf[(u & 1) * 8 + ks], oa[u]);
            }
            if (u > 0) {
                const f32x16& a = oa[u - 1];
#pragma unroll
                for (int i = 0; i < 2; ++i) {
                    u32x4 o = {pack2(a[8 * i], a[8 * i + 1]), pack2(a[8 * i + 2], a[8 * i + 3]), pack2(a[8 * i + 4], a[8 * i + 5]), pack2(a[8 * i + 6], a[8 * i + 7])};
                    ST16(o, lane * 16, WS_OP + (((qbg * 8 + 2 * w4 + ((u - 1) & 1)) * 8 + j) * 6 + ((u - 1) >> 1) * 2 + i) * 1024);
                }
            }
            if (u > 0 && u < 6) {
                __builtin_amdgcn_sched_group_barrier(0x008, 2, 0);
#pragma unroll
                for (int k = 0; k < 2; ++k) {
                    __builtin_amdgcn_sched_group_barrier(0x002, 2, 0); __builtin_amdgcn_sched_group_barrier(0x008, 1, 0);
                    __builtin_amdgcn_sched_group_barrier(0x002, 2, 0); __builtin_amdgcn_sched_group_barrier(0x008, 1, 0);
                    __builtin_amdgcn_sched_group_barrier(0x040, 1, 0); __builtin_amdgcn_sched_group_barrier(0x008, 1, 0);
                }
            }
            SB();
        }
        STAMP(14);
        drain();
        if (lane < 2) put_flag(ws, WS_IN2 + (qbg * 8 + 2 * w4 + lane) * 128 + j * 4, tag);
        __builtin_amdgcn_s_setprio(0);
        STAMP(15);
    }

    if (wave < 6) {
        const f32x4 gt0 = F4(lds, L_MOD + 3072 + d0 * 4), gt1 = F4(lds, L_MOD + 3072 + (d0 + 8) * 4);
        poll_flags(ws, WS_IN2 + cid * 128 + (lane & 7) * 4, tag);
        STAMP(16);
        u32x2 ml[8]; u32x4 pv[8];
#pragma unroll
        for (int k = 0; k < 8; ++k) ml[k] = __builtin_amdgcn_raw_buffer_load_b64(rs, (lane & 31) * 8, WS_ST + cid * 2048 + k * 256, 16);
#pragma unroll
        for (int k = 0; k < 8; ++k) pv[k] = LD16(lane * 16, WS_OP + ((cid * 8 + k) * 6 + unit) * 1024);
        SB();
        float m = __uint_as_float(ml[0][0]);
#pragma unroll
        for (int k = 1; k < 8; ++k) m = fmaxf(m, __uint_as_float(ml[k][0]));
        float wk[8], L = 0.f;
#pragma unroll
        for (int k = 0; k < 8; ++k) { wk[k] = __builtin_amdgcn_exp2f(__uint_as_float(ml[k][0]) - m); L = fmaf(wk[k], __uint_as_float(ml[k][1]), L); }
        const float inv = __builtin_amdgcn_rcpf(L);
        f32x2 o[4] = {};
#pragma unroll
        for (int k = 0; k < 8; ++k)
#pragma unroll
            for (int e = 0; e < 4; ++e) o[e] += unpack2(pv[k][e]) * wk[k];
        STAMP(17);
        u32x2 w0 = {pack2(o[0] * inv * gt0.xy + unpack2(xr[0][0])), pack2(o[1] * inv * gt0.zw + unpack2(xr[0][1]))};
        u32x2 w1 = {pack2(o[2] * inv * gt1.xy + unpack2(xr[1][0])), pack2(o[3] * inv * gt1.zw + unpack2(xr[1][1]))};
        *(u32x2*)(out + tok * D + d0) = w0;
        *(u32x2*)(out + tok * D + d0 + 8) = w1;
        if ((cid & 127) == 0 && tid == 0) put_flag(ws, WS_EPOCH + b * 4, tag);
        STAMP(18);
    }
    asm volatile(".Lpf_end:");
}

extern "C" void kernel_launch(void* const* d_in, const int* in_sizes, int n_in, void* d_out, int out_size, void* d_ws, size_t ws_size, hipStream_t stream) {
    dit_fused<<<256, 512, 0, stream>>>((const u16*)d_in[0], (const u16*)d_in[1], (const u16*)d_in[2], (const u16*)d_in[3], (const u16*)d_in[4], (u16*)d_out, (char*)d_ws);
}
```

```cpp
#include <hip/hip_runtime.h>
#include <hip/hip_bf16.h>

typedef unsigned short u16;
typedef unsigned long long u64;
typedef __bf16 bf16x2 __attribute__((ext_vector_type(2)));
typedef __bf16 bf16x8 __attribute__((ext_vector_type(8)));
typedef short s16x4 __attribute__((ext_vector_type(4)));
typedef float f32x2 __attribute__((ext_vector_type(2)));
typedef float f32x4 __attribute__((ext_vector_type(4)));
typedef float f32x16 __attribute__((ext_vector_type(16)));
typedef unsigned u32x2 __attribute__((ext_vector_type(2)));
typedef unsigned u32x4 __attribute__((ext_vector_type(4)));
#define GAS __attribute__((address_space(1)))
#define LAS __attribute__((address_space(3)))
#define DEV __device__ __forceinline__
#define RLX_AGENT __ATOMIC_RELAXED, __HIP_MEMORY_SCOPE_AGENT
#define SB() __builtin_amdgcn_sched_barrier(0)
#define UNROLL _Pragma("unroll")

constexpr int D = 384;
constexpr unsigned WS_EPOCH = 0;
constexpr unsigned WS_MODG = 256;
constexpr unsigned WS_IN0 = WS_MODG + 8 * 4608;
constexpr unsigned WS_IN1Q = WS_IN0 + 256 * 512;
constexpr unsigned WS_IN1V = WS_IN1Q + 256 * 128;
constexpr unsigned WS_IN2 = WS_IN1V + 256 * 512;
constexpr unsigned WS_MF = WS_IN2 + 256 * 128;
constexpr unsigned WS_QF = WS_MF + 2 * 294912;
constexpr unsigned WS_VF = WS_QF + 1572864;
constexpr unsigned WS_PF = WS_VF + 1572864;
constexpr unsigned WS_ML = WS_PF + 256 * 16384;
constexpr unsigned WS_END = WS_ML + 256 * 512;
constexpr unsigned WS_STAMP = WS_END;
constexpr int L_XK = 0;
constexpr int L_SA = 0, L_SB = 24576;
constexpr int L_H = 98304;
constexpr int L_MOD = 147456;
constexpr int L_KST = 152064;
constexpr int L_ML = 153088;
constexpr int L_TOTAL = 155136;
constexpr int PROW = 208;
constexpr int SPIN_CAP = 1 << 16;
constexpr int NWAVES = 8;

#define STAMP(i) do {} while (0)
#define STAMPC(i) do {} while (0)
#define STAMP_DECL do {} while (0)
#define STAMP_OUT do {} while (0)

DEV void wg_barrier() { asm volatile("s_waitcnt lgkmcnt(0)\n\ts_barrier" ::: "memory"); }
#define VMWAIT(n) asm volatile("s_waitcnt vmcnt(" #n ")" ::: "memory")
DEV unsigned pack2(float a, float b) { f32x2 f = {a, b}; return __builtin_bit_cast(unsigned, __builtin_convertvector(f, bf16x2)); }
DEV unsigned pack2(f32x2 f) { return __builtin_bit_cast(unsigned, __builtin_convertvector(f, bf16x2)); }
DEV float blo(unsigned u) { return __uint_as_float(u << 16); }
DEV float bhi(unsigned u) { return __uint_as_float(u & 0xffff0000u); }
DEV f32x2 unpack2(unsigned u) { return (f32x2){blo(u), bhi(u)}; }
DEV float dot2(unsigned a, unsigned b, float c) { return __builtin_amdgcn_fdot2_f32_bf16(__builtin_bit_cast(bf16x2, a), __builtin_bit_cast(bf16x2, b), c, false); }
DEV f32x4 mfma16(u32x4 a, u32x4 b, f32x4 c) { return __builtin_amdgcn_mfma_f32_16x16x32_bf16(__builtin_bit_cast(bf16x8, a), __builtin_bit_cast(bf16x8, b), c, 0, 0, 0); }
DEV f32x16 mfma32(u32x4 a, u32x4 b, f32x16 c) { return __builtin_amdgcn_mfma_f32_32x32x16_bf16(__builtin_bit_cast(bf16x8, a), __builtin_bit_cast(bf16x8, b), c, 0, 0, 0); }
DEV float rows_sum(float v) {
    auto a = __builtin_amdgcn_permlane16_swap(__float_as_uint(v), __float_as_uint(v), false, false);
    v = __uint_as_float(a[0]) + __uint_as_float(a[1]);
    auto b = __builtin_amdgcn_permlane32_swap(__float_as_uint(v), __float_as_uint(v), false, false);
    return __uint_as_float(b[0]) + __uint_as_float(b[1]);
}
DEV float rows_max(float v) {
    auto a = __builtin_amdgcn_permlane16_swap(__float_as_uint(v), __float_as_uint(v), false, false);
    v = fmaxf(__uint_as_float(a[0]), __uint_as_float(a[1]));
    auto b = __builtin_amdgcn_permlane32_swap(__float_as_uint(v), __float_as_uint(v), false, false);
    return fmaxf(__uint_as_float(b[0]), __uint_as_float(b[1]));
}
DEV float oct_sum(float v) {
    v += __int_as_float(__builtin_amdgcn_mov_dpp(__float_as_int(v), 0xB1, 0xf, 0xf, true));
    v += __int_as_float(__builtin_amdgcn_mov_dpp(__float_as_int(v), 0x4E, 0xf, 0xf, true));
    v += __int_as_float(__builtin_amdgcn_mov_dpp(__float_as_int(v), 0x141, 0xf, 0xf, true));
    return v;
}
DEV f32x2 ln_coef(float sum, float sq) {
    const float mu = sum * (1.f / D);
    const float rstd = __builtin_amdgcn_rsqf(fmaxf(sq * (1.f / D) - mu * mu, 0.f) + 1e-5f);
    return (f32x2){rstd, -mu * rstd};
}
DEV void poll_flags(char* ws, unsigned off, unsigned tag) {
    GAS unsigned* p = (GAS unsigned*)(ws + off);
#pragma unroll 1
    for (int spin = 0; spin < SPIN_CAP; ++spin) {
        unsigned v = __hip_atomic_load(p, RLX_AGENT);
        if (__all(v == tag)) break;
        __builtin_amdgcn_s_sleep(1);
    }
    asm volatile("" ::: "memory");
}
DEV void put_flag(char* ws, unsigned off, unsigned tag) { __hip_atomic_store((GAS unsigned*)(ws + off), tag, RLX_AGENT); }
DEV void glds(unsigned voff, const char* sbase, unsigned ldsdst) {
    asm volatile("s_mov_b32 m0, %2\n\ts_nop 0\n\tglobal_load_lds_dwordx4 %0, %1" :: "v"(voff), "s"(sbase), "s"(ldsdst) : "memory");
}
#define U4(p, off) (*(LAS u32x4*)((p) + (off)))
#define F4(p, off) (*(LAS f32x4*)((p) + (off)))
#define LD16(voff, soff) __builtin_amdgcn_raw_buffer_load_b128(rs, (voff), (soff), 16)
#define ST16(v, voff, soff) __builtin_amdgcn_raw_buffer_store_b128((v), rs, (voff), (soff), 16)
#define ST8(v, voff, soff) __builtin_amdgcn_raw_buffer_store_b64((v), rs, (voff), (soff), 16)
DEV LAS char* opaque(LAS char* p) { asm volatile("" : "+v"(p)); return p; }
#define FRAG(pe, po, t, s) U4(((s) & 1) ? (po) : (pe), ((t) * 12 + ((s) >> 1)) * 1024)

extern "C" __global__ __launch_bounds__(512, 2) void dit_fused(const u16* __restrict__ x, const u16* __restrict__ cvec, const u16* __restrict__ wqkv,
                                                               const u16* __restrict__ wo, const u16* __restrict__ wmod, u16* __restrict__ out, char* ws) {
    __shared__ __attribute__((aligned(16))) char smem[L_TOTAL];
    LAS char* lds = (LAS char*)smem;
    const unsigned lds0 = (unsigned)(size_t)lds;
    const int tid = threadIdx.x, lane = tid & 63;
    const int wave = __builtin_amdgcn_readfirstlane(tid >> 6);
    const int bid = blockIdx.x, xg = bid & 7, idx = bid >> 3;
    STAMP_DECL;
    STAMP(0); STAMPC(30);
    unsigned pf0;
    {
        u64 pc; unsigned len;
        asm volatile("s_getpc_b64 %0\n.Lpf_here:\n\ts_mov_b32 %1, .Lpf_end-.Lpf_here" : "=s"(pc), "=s"(len));
        unsigned o0 = min((unsigned)(((idx & 3) * 8 + wave) * 8 + (lane >> 3)) * 128u, len);
        asm volatile("global_load_dword %0, %1, %2" : "=&v"(pf0) : "v"(o0), "s"(pc) : "memory");
    }
    asm volatile("" :: "s"(x), "s"(cvec), "s"(wqkv), "s"(wo), "s"(wmod), "s"(out), "s"(ws));
    u64 ep;
    asm volatile("s_load_dwordx2 %0, %1, 0x0" : "=s"(ep) : "s"(ws) : "memory");

    const int b = xg >> 2;
    const int qbg = b * 16 + (xg & 3) * 4 + (idx >> 3);
    const int j = idx & 7;
    const int cid = qbg * 8 + j;
    const int r = lane & 15, g = lane >> 4, row8 = lane >> 3, sl = lane & 7;
    const unsigned swz = (unsigned)(sl ^ row8) * 16u;
    const int slice = xg + 8 * (idx - 18);
    const int role = idx < 9 ? 0 : idx < 18 ? 1 : slice < 18 ? 2 : 3;
    const int w4 = wave & 3;
    const int ti = idx - (role == 1 ? 9 : 0);
    const int t3 = (ti * 11) >> 5;
    const int s0 = 3 * (xg >> 1) + t3, cb = 3 * (xg & 1) + (ti - 3 * t3);

    unsigned craw = 0;
    if (role < 3) {
        if (role == 2) {
            asm volatile("global_load_dword %0, %1, %2" : "=v"(craw) : "v"(min(tid, 383) * 4), "s"(cvec) : "memory");
        } else {
            const int rg = wave >> 1, c0 = 3 * (wave & 1);
            const char* ap = (const char*)wqkv + (32 * s0 + 16 * (rg & 1) + 4 * (rg >> 1)) * 2304 + role * 1536 + c0 * 128;
            const unsigned va = (8 * (row8 >> 2) + (row8 & 3)) * 2304 + swz;
            UNROLL for (int i = 0; i < 3; ++i) glds(va, ap + i * 128, lds0 + L_SA + (rg * 6 + c0 + i) * 1024);
        }
        const int ldb = role == 0 ? 2304 : role == 1 ? 768 : 4608;
        const char* bp = role == 0 ? (const char*)wqkv + 768 + (64 * cb + 8 * wave) * 2304
                       : (role == 1 ? (const char*)wo + cb * 128 : (const char*)wmod + slice * 128) + 48 * wave * ldb;
        const int stepb = role == 0 ? 128 : 8 * ldb;
        const unsigned vb = row8 * ldb + swz;
        UNROLL for (int i = 0; i < 6; ++i) glds(vb, bp + i * stepb, lds0 + L_SB + (wave * 6 + i) * 1024);
    }
    u32x4 xq[6];
    {
        const char* xp = (const char*)x + (64 * qbg + 8 * wave) * 768;
        const unsigned vx = row8 * 768 + sl * 16;
        asm volatile("global_load_dwordx4 %0, %6, %7\n\tglobal_load_dwordx4 %1, %6, %7 offset:128\n\tglobal_load_dwordx4 %2, %6, %7 offset:256\n\t"
                     "global_load_dwordx4 %3, %6, %7 offset:384\n\tglobal_load_dwordx4 %4, %6, %7 offset:512\n\tglobal_load_dwordx4 %5, %6, %7 offset:640"
                     : "=&v"(xq[0]), "=&v"(xq[1]), "=&v"(xq[2]), "=&v"(xq[3]), "=&v"(xq[4]), "=&v"(xq[5]) : "v"(vx), "s"(xp) : "memory");
    }
    STAMP(1);
    const __amdgpu_buffer_rsrc_t rs = __builtin_amdgcn_make_buffer_rsrc(ws, 0, WS_END, 0x00020000);
    const int lpe = (r >> 3) * 6144 + (r & 7) * 128 + ((g ^ (r & 7)) * 16), lpo = lpe ^ 64;
    if (role == 2) {
        asm volatile("s_waitcnt vmcnt(12)" : "+v"(craw) :: "memory");
        float a = blo(craw), c = bhi(craw);
        a = a * __builtin_amdgcn_rcpf(1.f + __builtin_amdgcn_exp2f(-1.4426950408889634f * a));
        c = c * __builtin_amdgcn_rcpf(1.f + __builtin_amdgcn_exp2f(-1.4426950408889634f * c));
        const int row = tid >= 192, dw = tid - 192 * row, ch = dw >> 2;
        if (tid < 384) *(LAS unsigned*)(lds + L_SA + (ch >> 3) * 1024 + row * 128 + (((ch & 7) ^ row) * 16) + (dw & 3) * 4) = pack2(a, c);
    }
    u64 e_;
    asm volatile("s_waitcnt lgkmcnt(0)\n\ts_mov_b64 %0, %1" : "=s"(e_) : "s"(ep));
    const unsigned tag0 = (unsigned)e_ + 1u, tag1 = (unsigned)(e_ >> 32) + 1u, tag = b ? tag1 : tag0;

    const bool s0w = role < 3 && wave < 4;
    if (role < 3) {
        VMWAIT(6);
        wg_barrier();
        STAMP(2);
        if (wave < 4) {
            __builtin_amdgcn_s_setprio(2);
            u32x4 a0[12], a1[12], bf[12];
            LAS char* pe = opaque(lds + lpe); LAS char* po = opaque(lds + lpo);
            if (role == 0) {
                LAS char* qe = pe + L_SB + wave * 12288; LAS char* qo = po + L_SB + wave * 12288;
                UNROLL for (int s = 0; s < 12; ++s) { bf[s] = FRAG(qe, qo, 0, s); a0[s] = FRAG(pe, po, 0, s); a1[s] = FRAG(pe, po, 1, s); }
            } else {
                const int q = (lane >> 2) & 3, p = lane & 3;
                LAS char* t0 = opaque(lds + L_SB + g * 1024 + q * 128 + (((2 * wave + (p >> 1)) ^ q) * 16) + 8 * (p & 1));
                LAS char* t1 = opaque(lds + L_SB + g * 1024 + (4 + q) * 128 + (((2 * wave + (p >> 1)) ^ (4 + q)) * 16) + 8 * (p & 1));
                UNROLL for (int s = 0; s < 12; ++s) {
                    u32x2 l2 = __builtin_bit_cast(u32x2, __builtin_amdgcn_ds_read_tr16_b64_v4i16((LAS s16x4*)(t0 + s * 4096)));
                    u32x2 h2 = __builtin_bit_cast(u32x2, __builtin_amdgcn_ds_read_tr16_b64_v4i16((LAS s16x4*)(t1 + s * 4096)));
                    bf[s] = (u32x4){l2[0], l2[1], h2[0], h2[1]};
                    a0[s] = FRAG(pe, po, 0, s); a1[s] = FRAG(pe, po, 1, s);
                }
            }
            SB();
            f32x4 lo = {}, hi = {};
            UNROLL for (int s = 0; s < 12; ++s) { lo = mfma16(a0[s], bf[s], lo); hi = mfma16(a1[s], bf[s], hi); }
            if (role < 2) {
                u32x4 o = {pack2(lo.xy), pack2(lo.zw), pack2(hi.xy), pack2(hi.zw)};
                ST16(o, lane * 16, WS_MF + role * 294912 + ((4 * cb + wave) * 12 + s0) * 1024);
            } else {
                const unsigned u = pack2(lo.xy);
                const unsigned u0 = __shfl(u, 2 * sl), u1 = __shfl(u, 2 * sl + 1);
                const unsigned pay = lane < 32 ? ((u0 & 0xffffu) | (u1 << 16)) : ((u0 >> 16) | (u1 & 0xffff0000u));
                __hip_atomic_store((GAS u64*)(ws + WS_MODG + row8 * 4608 + (32 * slice + 8 * wave + sl) * 8), ((u64)(lane < 32 ? tag0 : tag1) << 32) | pay, RLX_AGENT);
            }
            __builtin_amdgcn_s_setprio(0);
            STAMP(3);
            wg_barrier();
            VMWAIT(1);
        }
    }
    if (!s0w) VMWAIT(0);
    asm volatile("" : "+v"(xq[0]), "+v"(xq[1]), "+v"(xq[2]), "+v"(xq[3]), "+v"(xq[4]), "+v"(xq[5]), "+v"(pf0));
    const unsigned vk = row8 * 768 + swz;
    const char* kp = (const char*)x + (b * 1024 + 128 * j + 64 * (wave >> 2)) * 768;
#define KEY_DMA() do { _Pragma("unroll 1") for (int rg = 0; rg < 8; ++rg) { \
        UNROLL for (int i = 0; i < 6; ++i) glds(vk, kp + rg * 6144 + i * 128, lds0 + L_XK + (((wave >> 2) * 8 + rg) * 6 + i) * 1024); } } while (0)
    if (wave == 3) KEY_DMA();
    f32x2 n[24];
    {
        float s0_ = 0.f, s1_ = 0.f, q0_ = 0.f, q1_ = 0.f;
        UNROLL for (int i = 0; i < 6; ++i) {
            s0_ = dot2(xq[i][0], 0x3f803f80u, s0_); q0_ = dot2(xq[i][0], xq[i][0], q0_); s1_ = dot2(xq[i][1], 0x3f803f80u, s1_); q1_ = dot2(xq[i][1], xq[i][1], q1_);
            s0_ = dot2(xq[i][2], 0x3f803f80u, s0_); q0_ = dot2(xq[i][2], xq[i][2], q0_); s1_ = dot2(xq[i][3], 0x3f803f80u, s1_); q1_ = dot2(xq[i][3], xq[i][3], q1_);
        }
        const f32x2 st = ln_coef(oct_sum(s0_ + s1_), oct_sum(q0_ + q1_));
        UNROLL for (int i = 0; i < 6; ++i)
            UNROLL for (int e = 0; e < 4; ++e) n[4 * i + e] = unpack2(xq[i][e]) * st[0] + st[1];
        UNROLL for (int i = 0; i < 24; ++i) asm volatile("" : "+v"(n[i]));
    }
    STAMP(4);
    if (wave < 4) {
        if (role < 2) {
            if (wave == 3) VMWAIT(48); else VMWAIT(0);
            const int ctg = 4 * cb + wave, jc = (ctg * 11) >> 5;
            if (lane < 32) put_flag(ws, WS_IN0 + (lane * 8 + jc) * 512 + role * 256 + ((ctg - 3 * jc) * 12 + s0) * 4, lane < 16 ? tag0 : tag1);
        }
    } else {
        if (role < 3) wg_barrier();
        if (wave == 7) KEY_DMA();
        else {
            GAS u64* gp = (GAS u64*)(ws + WS_MODG + xg * 4608) + w4 * 192 + lane;
            u64 g0, g1, g2;
#pragma unroll 1
            for (int spin = 0; spin < SPIN_CAP; ++spin) {
                g0 = __hip_atomic_load(gp, RLX_AGENT); g1 = __hip_atomic_load(gp + 64, RLX_AGENT); g2 = __hip_atomic_load(gp + 128, RLX_AGENT);
                if (__all((unsigned)(g0 >> 32) == tag && (unsigned)(g1 >> 32) == tag && (unsigned)(g2 >> 32) == tag)) break;
                __builtin_amdgcn_s_sleep(1);
            }
            const float add = w4 == 1 ? 1.f : 0.f;
            LAS char* pm = lds + L_MOD + w4 * 1536 + lane * 8;
            *(LAS f32x2*)(pm) = unpack2((unsigned)g0) + add;
            *(LAS f32x2*)(pm + 512) = unpack2((unsigned)g1) + add;
            *(LAS f32x2*)(pm + 1024) = unpack2((unsigned)g2) + add;
        }
    }
    STAMP(5);
    wg_barrier();
    STAMP(6);
    const unsigned f0off = WS_IN0 + cid * 512 + (wave < 4 ? 256 : 0) + w4 * 48 + min(lane, 11) * 4;
    unsigned f0 = tag;
    if (w4 < 3) f0 = __hip_atomic_load((GAS unsigned*)(ws + f0off), RLX_AGENT);
    SB();
    {
        LAS char* pm = opaque(lds + L_MOD + sl * 32);
        LAS char* ph = opaque(lds + L_H + wave * 6144 + row8 * 128 + swz);
        UNROLL for (int i = 0; i < 6; ++i) {
            const f32x4 t0 = F4(pm, i * 256), t1 = F4(pm, i * 256 + 16), c0 = F4(pm, i * 256 + 1536), c1 = F4(pm, i * 256 + 1536 + 16);
            u32x4 o;
            o[0] = pack2(n[4 * i + 0] * c0.xy + t0.xy);
            o[1] = pack2(n[4 * i + 1] * c0.zw + t0.zw);
            o[2] = pack2(n[4 * i + 2] * c1.xy + t1.xy);
            o[3] = pack2(n[4 * i + 3] * c1.zw + t1.zw);
            U4(ph, i * 1024) = o;
        }
    }
    STAMP(7);

    const int ct = 3 * j + w4;
#define STAGE1_GEMM(MBASE, MFMA_EXPR)                                                                  \
        if (!__all(f0 == tag)) poll_flags(ws, f0off, tag);                                             \
        asm volatile("" ::: "memory");                                                                 \
        STAMP(8);                                                                                      \
        u32x4 mf[12], hf[12][4];                                                                       \
        UNROLL for (int s = 0; s < 12; ++s) mf[s] = LD16(lane * 16, (MBASE) + s * 1024);               \
        SB();                                                                                          \
        wg_barrier();                                                          \
        LAS char* pe = opaque(lds + L_H + lpe); LAS char* po = opaque(lds + L_H + lpo);                \
        UNROLL for (int s = 0; s < 8; ++s)                                                             \
            UNROLL for (int tt = 0; tt < 4; ++tt) hf[s][tt] = FRAG(pe, po, tt, s);                     \
        SB();                                                                                          \
        f32x4 ac[4] = {};                                                                              \
        UNROLL for (int s = 0; s < 12; ++s) {                                                          \
            UNROLL for (int tt = 0; tt < 4; ++tt) {                                                    \
                ac[tt] = MFMA_EXPR;                                                                    \
                if (s + 8 < 12) hf[s + 8][tt] = FRAG(pe, po, tt, s + 8);                               \
                SB();                                                                                  \
            }                                                                                          \
        }
    if (w4 == 3) {
        wg_barrier();
        VMWAIT(0);
        STAMP(8);
        const u32x4 ones = {0x3f803f80u, 0x3f803f80u, 0x3f803f80u, 0x3f803f80u};
#pragma unroll 1
        for (int t = 0; t < 4; ++t) {
            const int tile = (wave >> 2) * 4 + t;
            LAS char* pe = lds + L_XK + tile * 12288 + lpe; LAS char* po = lds + L_XK + tile * 12288 + lpo;
            u32x4 f[12];
            UNROLL for (int s = 0; s < 12; ++s) f[s] = FRAG(pe, po, 0, s);
            SB();
            f32x4 sm = {}, gr = {};
            UNROLL for (int s = 0; s < 12; ++s) { sm = mfma16(ones, f[s], sm); gr = mfma16(f[s], f[s], gr); }
            const float d = (r & 2) ? ((r & 1) ? gr[3] : gr[2]) : ((r & 1) ? gr[1] : gr[0]);
            const f32x2 st = ln_coef(sm[0], d);
            if (g == (r >> 2)) { *(LAS float*)(lds + L_KST + (tile * 16 + r) * 4) = st[0]; *(LAS float*)(lds + L_KST + 512 + (tile * 16 + r) * 4) = st[1]; }
        }
        STAMP(9);
    } else if (wave > 3) {
        __builtin_amdgcn_s_setprio(2);
        const f32x4 qs = F4(lds, L_MOD + 1536 + (16 * ct + 4 * g) * 4) * (1.4426950408889634f * 0.05103103630798288f);
        STAGE1_GEMM(WS_MF + ct * 12288, mfma16(mf[s], hf[s][tt], ac[tt]))
        const unsigned vo = ((2 * (ct & 1) + (g >> 1)) * 16 + r) * 16 + (g & 1) * 8;
        UNROLL for (int tt = 0; tt < 4; ++tt) {
            const f32x4 v = ac[tt] * qs;
            u32x2 o = {pack2(v.xy), pack2(v.zw)};
            ST8(o, vo, WS_QF + ((4 * qbg + tt) * 12 + (ct >> 1)) * 1024);
        }
        __builtin_amdgcn_s_setprio(0);
        STAMP(9);
        VMWAIT(0);
        if (lane < 8) put_flag(ws, WS_IN1Q + (qbg * 8 + lane) * 128 + ct * 4, tag);
    } else {
        STAGE1_GEMM(WS_MF + 294912 + ct * 12288, mfma16(hf[s][tt], mf[s], ac[tt]))
        const int vw = j >> 1, vc = ct - vw * 6;
        const unsigned vo = (g >> 1) * 1024 + ((g & 1) * 32 + 16 * (ct & 1) + r) * 16;
        UNROLL for (int tp = 0; tp < 2; ++tp) {
            u32x4 o = {pack2(ac[2 * tp].xy), pack2(ac[2 * tp].zw), pack2(ac[2 * tp + 1].xy), pack2(ac[2 * tp + 1].zw)};
            ST16(o, vo, WS_VF + ((((qbg >> 1) * 4 + vw) * 3 + (vc >> 1)) * 8 + 4 * (qbg & 1) + 2 * tp) * 1024);
        }
        STAMP(9);
        VMWAIT(0);
        if (lane < 32) put_flag(ws, WS_IN1V + ((b * 16 + (lane >> 1)) * 8 + 2 * vw + (lane & 1)) * 512 + ((qbg >> 1) & 7) * 64 + ((qbg & 1) * 6 + vc) * 4, tag);
    }
    STAMP(10);
    wg_barrier();
    STAMP(11);

    if (wave > 3) {
        LAS char* pe0 = opaque(lds + L_XK + lpe); LAS char* po0 = opaque(lds + L_XK + lpo);
        LAS char* pe1 = opaque(lds + L_XK + 49152 + lpe); LAS char* po1 = opaque(lds + L_XK + 49152 + lpo);
        u32x4 kf[12][8], qf[12];
        f32x4 krs[8], kmu[8];
#define KF(s, t) ((t) < 4 ? FRAG(pe0, po0, (t), (s)) : FRAG(pe1, po1, (t) - 4, (s)))
        UNROLL for (int s = 0; s < 4; ++s)
            UNROLL for (int t = 0; t < 8; ++t) kf[s][t] = KF(s, t);
        LAS char* pst = opaque(lds + L_KST + g * 16);
        SB();
        poll_flags(ws, WS_IN1Q + cid * 128 + min(lane, 23) * 4, tag);
        __builtin_amdgcn_s_setprio(2);
        STAMP(12);
        UNROLL for (int s = 0; s < 12; ++s) qf[s] = LD16(lane * 16, WS_QF + ((4 * qbg + w4) * 12 + s) * 1024);
        SB();
        f32x4 sa[8] = {};
        float rq0 = 0.f, rq1 = 0.f;
        UNROLL for (int s = 0; s < 12; ++s) {
            UNROLL for (int t = 0; t < 8; ++t) {
                sa[t] = mfma16(kf[s][t], qf[s], sa[t]);
                if (s + 4 < 12) kf[s + 4][t] = KF(s + 4, t);
                else if (s == 9) krs[t] = F4(pst, t * 64);
                else if (s == 10) kmu[t] = F4(pst, 512 + t * 64);
                if (t == 0) rq0 = dot2(qf[s][0], 0x3f803f80u, rq0);
                if (t == 1) rq1 = dot2(qf[s][1], 0x3f803f80u, rq1);
                if (t == 2) rq0 = dot2(qf[s][2], 0x3f803f80u, rq0);
                if (t == 3) rq1 = dot2(qf[s][3], 0x3f803f80u, rq1);
                if (s == 11 && t == 4) rq0 = rows_sum(rq0 + rq1);
                SB();
            }
        }
        STAMP(13);
        float mx = -3.0e38f;
        UNROLL for (int t = 0; t < 8; ++t) {
            sa[t] = sa[t] * krs[t] + kmu[t] * rq0;
            mx = fmaxf(fmaxf(fmaxf(mx, sa[t][0]), sa[t][1]), fmaxf(sa[t][2], sa[t][3]));
        }
        mx = rows_max(mx);
        f32x4 ls = {};
        const unsigned vo = (g >> 1) * 1024 + ((g & 1) * 32 + (w4 & 1) * 16 + r) * 16;
        UNROLL for (int tp = 0; tp < 4; ++tp) {
            UNROLL for (int t = 2 * tp; t < 2 * tp + 2; ++t) {
                sa[t] -= mx;
                UNROLL for (int e = 0; e < 4; ++e) sa[t][e] = __builtin_amdgcn_exp2f(sa[t][e]);
                ls += sa[t];
            }
            u32x4 o = {pack2(sa[2 * tp].xy), pack2(sa[2 * tp].zw), pack2(sa[2 * tp + 1].xy), pack2(sa[2 * tp + 1].zw)};
            ST16(o, vo, WS_PF + ((cid * 2 + (w4 >> 1)) * 8 + 2 * tp) * 1024);
        }
        const float l = rows_sum((ls[0] + ls[1]) + (ls[2] + ls[3]));
        const u32x2 ml = {__float_as_uint(mx), __float_as_uint(l)};
        if (g == 0) ST8(ml, r * 8, WS_ML + cid * 512 + w4 * 128);
        __builtin_amdgcn_s_setprio(0);
        STAMP(14);
        wg_barrier();
    } else {
        poll_flags(ws, WS_IN1V + cid * 512 + (wave + 4 * (lane >> 5)) * 64 + min(lane & 31, 11) * 4, tag);
        STAMP(12);
    }

    u32x4 vf[24];
    UNROLL for (int i = 0; i < 24; ++i) vf[i] = LD16(lane * 16, WS_VF + (((b * 8 + wave) * 4 + (j >> 1)) * 24 + i) * 1024);
    SB();
    if (wave > 3) {
        VMWAIT(24);
        if (lane < 4) put_flag(ws, WS_IN2 + (qbg * 8 + 2 * lane + (w4 >> 1)) * 128 + (j * 2 + (w4 & 1)) * 4, tag);
        STAMP(15);
    } else wg_barrier();
    const int oq = (tid * 43691) >> 19, oc = tid - 12 * oq;
    const int ooff = (64 * qbg + 32 * (j & 1) + oq) * D + 96 * (j >> 1) + 8 * oc;
    u32x4 xr = {};
    if (wave < 6) xr = *(const u32x4*)(x + ooff);
    poll_flags(ws, WS_IN2 + cid * 128 + (wave * 2 + (lane & 1)) * 4, tag);
    STAMP(16);
    {
        u32x4 pf[8];
        const int prod = qbg * 8 + wave;
        UNROLL for (int ks = 0; ks < 8; ++ks) pf[ks] = LD16(lane * 16, WS_PF + ((prod * 2 + (j & 1)) * 8 + ks) * 1024);
        const u32x2 ml = __builtin_amdgcn_raw_buffer_load_b64(rs, (lane & 31) * 8, WS_ML + prod * 512 + (j & 1) * 256, 16);
        SB();
        LAS char* pw = lds + L_XK + (wave * 32 + (lane & 31)) * PROW + (lane >> 5) * 16;
        f32x16 oa[3];
        UNROLL for (int dt = 0; dt < 4; ++dt) {
            if (dt < 3) {
                oa[dt] = (f32x16){};
                UNROLL for (int ks = 0; ks < 8; ++ks) oa[dt] = mfma32(vf[dt * 8 + ks], pf[ks], oa[dt]);
            }
            if (dt > 0) {
                const f32x16& a = oa[dt - 1];
                UNROLL for (int i = 0; i < 2; ++i) {
                    auto r0 = __builtin_amdgcn_permlane32_swap(pack2(a[8 * i], a[8 * i + 1]), pack2(a[8 * i + 4], a[8 * i + 5]), false, false);
                    auto r1 = __builtin_amdgcn_permlane32_swap(pack2(a[8 * i + 2], a[8 * i + 3]), pack2(a[8 * i + 6], a[8 * i + 7]), false, false);
                    U4(pw, (dt - 1) * 64 + i * 32) = (u32x4){r0[0], r1[0], r0[1], r1[1]};
                }
            }
            if (dt == 1) { if (lane < 32) *(LAS u32x2*)(lds + L_ML + (wave * 32 + lane) * 8) = ml; }
            if (dt > 0 && dt < 3) {
                __builtin_amdgcn_sched_group_barrier(0x008, 2, 0);
                UNROLL for (int k = 0; k < 6; ++k) { __builtin_amdgcn_sched_group_barrier(0x282, 3, 0); __builtin_amdgcn_sched_group_barrier(0x008, 1, 0); }
            }
            SB();
        }
    }
    STAMP(17);
    wg_barrier();
    STAMP(18);
    if (wave < 6) {
        f32x2 ml[8]; u32x4 pv[8];
        UNROLL for (int k = 0; k < 8; ++k) ml[k] = *(LAS f32x2*)(lds + L_ML + (k * 32 + oq) * 8);
        UNROLL for (int k = 0; k < 8; ++k) pv[k] = U4(lds, L_XK + (k * 32 + oq) * PROW + oc * 16);
        const f32x4 gt0 = F4(lds, L_MOD + 3072 + (96 * (j >> 1) + 8 * oc) * 4), gt1 = F4(lds, L_MOD + 3072 + (96 * (j >> 1) + 8 * oc) * 4 + 16);
        SB();
        float m = ml[0][0];
        UNROLL for (int k = 1; k < 8; ++k) m = fmaxf(m, ml[k][0]);
        float wk[8], L = 0.f;
        UNROLL for (int k = 0; k < 8; ++k) { wk[k] = __builtin_amdgcn_exp2f(ml[k][0] - m); L = fmaf(wk[k], ml[k][1], L); }
        const float inv = __builtin_amdgcn_rcpf(L);
        f32x2 o[4] = {};
        UNROLL for (int k = 0; k < 8; ++k)
            UNROLL for (int e = 0; e < 4; ++e) o[e] += unpack2(pv[k][e]) * wk[k];
        u32x4 w = {pack2(o[0] * inv * gt0.xy + unpack2(xr[0])), pack2(o[1] * inv * gt0.zw + unpack2(xr[1])),
                   pack2(o[2] * inv * gt1.xy + unpack2(xr[2])), pack2(o[3] * inv * gt1.zw + unpack2(xr[3]))};
        *(u32x4*)(out + ooff) = w;
        if ((cid & 127) == 0 && tid == 0) put_flag(ws, WS_EPOCH + b * 4, tag);
    }
    STAMP(29); STAMPC(31);
    STAMP_OUT;
    asm volatile(".Lpf_end:");
}

extern "C" void kernel_launch(void* const* d_in, const int* in_sizes, int n_in, void* d_out, int out_size, void* d_ws, size_t ws_size, hipStream_t stream) {
    dit_fused<<<256, 512, 0, stream>>>((const u16*)d_in[0], (const u16*)d_in[1], (const u16*)d_in[2], (const u16*)d_in[3], (const u16*)d_in[4], (u16*)d_out, (char*)d_ws);
}
```

```cpp
#define AMP 2
#include <hip/hip_runtime.h>
#include <hip/hip_bf16.h>

typedef unsigned short u16;
typedef unsigned long long u64;
typedef __bf16 bf16x2 __attribute__((ext_vector_type(2)));
typedef __bf16 bf16x8 __attribute__((ext_vector_type(8)));
typedef short s16x4 __attribute__((ext_vector_type(4)));
typedef float f32x2 __attribute__((ext_vector_type(2)));
typedef float f32x4 __attribute__((ext_vector_type(4)));
typedef float f32x16 __attribute__((ext_vector_type(16)));
typedef unsigned u32x2 __attribute__((ext_vector_type(2)));
typedef unsigned u32x4 __attribute__((ext_vector_type(4)));
#define GAS __attribute__((address_space(1)))
#define LAS __attribute__((address_space(3)))
#define DEV __device__ __forceinline__
#define RLX_AGENT __ATOMIC_RELAXED, __HIP_MEMORY_SCOPE_AGENT
#define SB() __builtin_amdgcn_sched_barrier(0)
#define UNROLL _Pragma("unroll")

constexpr int D = 384;
constexpr unsigned WS_EPOCH = 0;
constexpr unsigned WS_MODG = 256;
constexpr unsigned WS_IN0 = WS_MODG + 8 * 4608;
constexpr unsigned WS_IN1Q = WS_IN0 + 256 * 512;
constexpr unsigned WS_IN1V = WS_IN1Q + 256 * 128;
constexpr unsigned WS_IN2 = WS_IN1V + 256 * 512;
constexpr unsigned WS_MF = WS_IN2 + 256 * 128;
constexpr unsigned WS_QF = WS_MF + 2 * 294912;
constexpr unsigned WS_VF = WS_QF + 1572864;
constexpr unsigned WS_PF = WS_VF + 1572864;
constexpr unsigned WS_ML = WS_PF + 256 * 16384;
constexpr unsigned WS_END = WS_ML + 256 * 512;
constexpr unsigned WS_STAMP = WS_END;
constexpr int L_XK = 0;
constexpr int L_SA = 0, L_SB = 24576;
constexpr int L_H = 98304;
constexpr int L_MOD = 147456;
constexpr int L_KST = 152064;
constexpr int L_ML = 153088;
constexpr int L_TOTAL = 155136;
constexpr int PROW = 208;
constexpr int SPIN_CAP = 1 << 16;
constexpr int NWAVES = 8;

#define STAMP(i) do { if ((i) == 0 || (i) == AMP) { SB(); unsigned t_ = (unsigned)__builtin_amdgcn_s_memrealtime(); if ((i) == 0) amp0 = t_; else amp1 = t_; SB(); } } while (0)
#define STAMPC(i) do {} while (0)
#define STAMP_DECL unsigned amp0 = 0, amp1 = 0
#define STAMP_OUT do { if (wave == 4 && amp1 != 0) { unsigned te = (unsigned)__builtin_amdgcn_s_memrealtime(); unsigned d = (amp1 - amp0) * 10u; if (d > 100000u) d = 0; while ((unsigned)__builtin_amdgcn_s_memrealtime() - te < d) __builtin_amdgcn_s_sleep(4); } } while (0)

DEV void wg_barrier() { asm volatile("s_waitcnt lgkmcnt(0)\n\ts_barrier" ::: "memory"); }
#define VMWAIT(n) asm volatile("s_waitcnt vmcnt(" #n ")" ::: "memory")
DEV unsigned pack2(float a, float b) { f32x2 f = {a, b}; return __builtin_bit_cast(unsigned, __builtin_convertvector(f, bf16x2)); }
DEV unsigned pack2(f32x2 f) { return __builtin_bit_cast(unsigned, __builtin_convertvector(f, bf16x2)); }
DEV float blo(unsigned u) { return __uint_as_float(u << 16); }
DEV float bhi(unsigned u) { return __uint_as_float(u & 0xffff0000u); }
DEV f32x2 unpack2(unsigned u) { return (f32x2){blo(u), bhi(u)}; }
DEV float dot2(unsigned a, unsigned b, float c) { return __builtin_amdgcn_fdot2_f32_bf16(__builtin_bit_cast(bf16x2, a), __builtin_bit_cast(bf16x2, b), c, false); }
DEV f32x4 mfma16(u32x4 a, u32x4 b, f32x4 c) { return __builtin_amdgcn_mfma_f32_16x16x32_bf16(__builtin_bit_cast(bf16x8, a), __builtin_bit_cast(bf16x8, b), c, 0, 0, 0); }
DEV f32x16 mfma32(u32x4 a, u32x4 b, f32x16 c) { return __builtin_amdgcn_mfma_f32_32x32x16_bf16(__builtin_bit_cast(bf16x8, a), __builtin_bit_cast(bf16x8, b), c, 0, 0, 0); }
DEV float rows_sum(float v) {
    auto a = __builtin_amdgcn_permlane16_swap(__float_as_uint(v), __float_as_uint(v), false, false);
    v = __uint_as_float(a[0]) + __uint_as_float(a[1]);
    auto b = __builtin_amdgcn_permlane32_swap(__float_as_uint(v), __float_as_uint(v), false, false);
    return __uint_as_float(b[0]) + __uint_as_float(b[1]);
}
DEV float rows_max(float v) {
    auto a = __builtin_amdgcn_permlane16_swap(__float_as_uint(v), __float_as_uint(v), false, false);
    v = fmaxf(__uint_as_float(a[0]), __uint_as_float(a[1]));
    auto b = __builtin_amdgcn_permlane32_swap(__float_as_uint(v), __float_as_uint(v), false, false);
    return fmaxf(__uint_as_float(b[0]), __uint_as_float(b[1]));
}
DEV float oct_sum(float v) {
    v += __int_as_float(__builtin_amdgcn_mov_dpp(__float_as_int(v), 0xB1, 0xf, 0xf, true));
    v += __int_as_float(__builtin_amdgcn_mov_dpp(__float_as_int(v), 0x4E, 0xf, 0xf, true));
    v += __int_as_float(__builtin_amdgcn_mov_dpp(__float_as_int(v), 0x141, 0xf, 0xf, true));
    return v;
}
DEV f32x2 ln_coef(float sum, float sq) {
    const float mu = sum * (1.f / D);
    const float rstd = __builtin_amdgcn_rsqf(fmaxf(sq * (1.f / D) - mu * mu, 0.f) + 1e-5f);
    return (f32x2){rstd, -mu * rstd};
}
DEV void poll_flags(char* ws, unsigned off, unsigned tag) {
    GAS unsigned* p = (GAS unsigned*)(ws + off);
#pragma unroll 1
    for (int spin = 0; spin < SPIN_CAP; ++spin) {
        unsigned v = __hip_atomic_load(p, RLX_AGENT);
        if (__all(v == tag)) break;
        __builtin_amdgcn_s_sleep(1);
    }
    asm volatile("" ::: "memory");
}
DEV void put_flag(char* ws, unsigned off, unsigned tag) { __hip_atomic_store((GAS unsigned*)(ws + off), tag, RLX_AGENT); }
DEV void glds(unsigned voff, const char* sbase, unsigned ldsdst) {
    asm volatile("s_mov_b32 m0, %2\n\ts_nop 0\n\tglobal_load_lds_dwordx4 %0, %1" :: "v"(voff), "s"(sbase), "s"(ldsdst) : "memory");
}
#define U4(p, off) (*(LAS u32x4*)((p) + (off)))
#define F4(p, off) (*(LAS f32x4*)((p) + (off)))
#define LD16(voff, soff) __builtin_amdgcn_raw_buffer_load_b128(rs, (voff), (soff), 16)
#define ST16(v, voff, soff) __builtin_amdgcn_raw_buffer_store_b128((v), rs, (voff), (soff), 16)
#define ST8(v, voff, soff) __builtin_amdgcn_raw_buffer_store_b64((v), rs, (voff), (soff), 16)
DEV LAS char* opaque(LAS char* p) { asm volatile("" : "+v"(p)); return p; }
#define FRAG(pe, po, t, s) U4(((s) & 1) ? (po) : (pe), ((t) * 12 + ((s) >> 1)) * 1024)

extern "C" __global__ __launch_bounds__(512, 2) void dit_fused(const u16* __restrict__ x, const u16* __restrict__ cvec, const u16* __restrict__ wqkv,
                                                               const u16* __restrict__ wo, const u16* __restrict__ wmod, u16* __restrict__ out, char* ws) {
    __shared__ __attribute__((aligned(16))) char smem[L_TOTAL];
    LAS char* lds = (LAS char*)smem;
    const unsigned lds0 = (unsigned)(size_t)lds;
    const int tid = threadIdx.x, lane = tid & 63;
    const int wave = __builtin_amdgcn_readfirstlane(tid >> 6);
    const int bid = blockIdx.x, xg = bid & 7, idx = bid >> 3;
    STAMP_DECL;
    STAMP(0); STAMPC(30);
    unsigned pf0;
    {
        u64 pc; unsigned len;
        asm volatile("s_getpc_b64 %0\n.Lpf_here:\n\ts_mov_b32 %1, .Lpf_end-.Lpf_here" : "=s"(pc), "=s"(len));
        unsigned o0 = min((unsigned)(((idx & 3) * 8 + wave) * 8 + (lane >> 3)) * 128u, len);
        asm volatile("global_load_dword %0, %1, %2" : "=&v"(pf0) : "v"(o0), "s"(pc) : "memory");
    }
    asm volatile("" :: "s"(x), "s"(cvec), "s"(wqkv), "s"(wo), "s"(wmod), "s"(out), "s"(ws));
    u64 ep;
    asm volatile("s_load_dwordx2 %0, %1, 0x0" : "=s"(ep) : "s"(ws) : "memory");

    const int b = xg >> 2;
    const int qbg = b * 16 + (xg & 3) * 4 + (idx >> 3);
    const int j = idx & 7;
    const int cid = qbg * 8 + j;
    const int r = lane & 15, g = lane >> 4, row8 = lane >> 3, sl = lane & 7;
    const unsigned swz = (unsigned)(sl ^ row8) * 16u;
    const int slice = xg + 8 * (idx - 18);
    const int role = idx < 9 ? 0 : idx < 18 ? 1 : slice < 18 ? 2 : 3;
    const int w4 = wave & 3;
    const int ti = idx - (role == 1 ? 9 : 0);
    const int t3 = (ti * 11) >> 5;
    const int s0 = 3 * (xg >> 1) + t3, cb = 3 * (xg & 1) + (ti - 3 * t3);

    unsigned craw = 0;
    if (role < 3) {
        if (role == 2) {
            asm volatile("global_load_dword %0, %1, %2" : "=v"(craw) : "v"(min(tid, 383) * 4), "s"(cvec) : "memory");
        } else {
            const int rg = wave >> 1, c0 = 3 * (wave & 1);
            const char* ap = (const char*)wqkv + (32 * s0 + 16 * (rg & 1) + 4 * (rg >> 1)) * 2304 + role * 1536 + c0 * 128;
            const unsigned va = (8 * (row8 >> 2) + (row8 & 3)) * 2304 + swz;
            UNROLL for (int i = 0; i < 3; ++i) glds(va, ap + i * 128, lds0 + L_SA + (rg * 6 + c0 + i) * 1024);
        }
        const int ldb = role == 0 ? 2304 : role == 1 ? 768 : 4608;
        const char* bp = role == 0 ? (const char*)wqkv + 768 + (64 * cb + 8 * wave) * 2304
                       : (role == 1 ? (const char*)wo + cb * 128 : (const char*)wmod + slice * 128) + 48 * wave * ldb;
        const int stepb = role == 0 ? 128 : 8 * ldb;
        const unsigned vb = row8 * ldb + swz;
        UNROLL for (int i = 0; i < 6; ++i) glds(vb, bp + i * stepb, lds0 + L_SB + (wave * 6 + i) * 1024);
    }
    u32x4 xq[6];
    {
        const char* xp = (const char*)x + (64 * qbg + 8 * wave) * 768;
        const unsigned vx = row8 * 768 + sl * 16;
        asm volatile("global_load_dwordx4 %0, %6, %7\n\tglobal_load_dwordx4 %1, %6, %7 offset:128\n\tglobal_load_dwordx4 %2, %6, %7 offset:256\n\t"
                     "global_load_dwordx4 %3, %6, %7 offset:384\n\tglobal_load_dwordx4 %4, %6, %7 offset:512\n\tglobal_load_dwordx4 %5, %6, %7 offset:640"
                     : "=&v"(xq[0]), "=&v"(xq[1]), "=&v"(xq[2]), "=&v"(xq[3]), "=&v"(xq[4]), "=&v"(xq[5]) : "v"(vx), "s"(xp) : "memory");
    }
    STAMP(1);
    const __amdgpu_buffer_rsrc_t rs = __builtin_amdgcn_make_buffer_rsrc(ws, 0, WS_END, 0x00020000);
    const int lpe = (r >> 3) * 6144 + (r & 7) * 128 + ((g ^ (r & 7)) * 16), lpo = lpe ^ 64;
    if (role == 2) {
        asm volatile("s_waitcnt vmcnt(12)" : "+v"(craw) :: "memory");
        float a = blo(craw), c = bhi(craw);
        a = a * __builtin_amdgcn_rcpf(1.f + __builtin_amdgcn_exp2f(-1.4426950408889634f * a));
        c = c * __builtin_amdgcn_rcpf(1.f + __builtin_amdgcn_exp2f(-1.4426950408889634f * c));
        const int row = tid >= 192, dw = tid - 192 * row, ch = dw >> 2;
        if (tid < 384) *(LAS unsigned*)(lds + L_SA + (ch >> 3) * 1024 + row * 128 + (((ch & 7) ^ row) * 16) + (dw & 3) * 4) = pack2(a, c);
    }
    u64 e_;
    asm volatile("s_waitcnt lgkmcnt(0)\n\ts_mov_b64 %0, %1" : "=s"(e_) : "s"(ep));
    const unsigned tag0 = (unsigned)e_ + 1u, tag1 = (unsigned)(e_ >> 32) + 1u, tag = b ? tag1 : tag0;

    const bool s0w = role < 3 && wave < 4;
    if (role < 3) {
        VMWAIT(6);
        wg_barrier();
        STAMP(2);
        if (wave < 4) {
            __builtin_amdgcn_s_setprio(2);
            u32x4 a0[12], a1[12], bf[12];
            LAS char* pe = opaque(lds + lpe); LAS char* po = opaque(lds + lpo);
            if (role == 0) {
                LAS char* qe = pe + L_SB + wave * 12288; LAS char* qo = po + L_SB + wave * 12288;
                UNROLL for (int s = 0; s < 12; ++s) { bf[s] = FRAG(qe, qo, 0, s); a0[s] = FRAG(pe, po, 0, s); a1[s] = FRAG(pe, po, 1, s); }
            } else {
                const int q = (lane >> 2) & 3, p = lane & 3;
                LAS char* t0 = opaque(lds + L_SB + g * 1024 + q * 128 + (((2 * wave + (p >> 1)) ^ q) * 16) + 8 * (p & 1));
                LAS char* t1 = opaque(lds + L_SB + g * 1024 + (4 + q) * 128 + (((2 * wave + (p >> 1)) ^ (4 + q)) * 16) + 8 * (p & 1));
                UNROLL for (int s = 0; s < 12; ++s) {
                    u32x2 l2 = __builtin_bit_cast(u32x2, __builtin_amdgcn_ds_read_tr16_b64_v4i16((LAS s16x4*)(t0 + s * 4096)));
                    u32x2 h2 = __builtin_bit_cast(u32x2, __builtin_amdgcn_ds_read_tr16_b64_v4i16((LAS s16x4*)(t1 + s * 4096)));
                    bf[s] = (u32x4){l2[0], l2[1], h2[0], h2[1]};
                    a0[s] = FRAG(pe, po, 0, s); a1[s] = FRAG(pe, po, 1, s);
                }
            }
            SB();
            f32x4 lo = {}, hi = {};
            UNROLL for (int s = 0; s < 12; ++s) { lo = mfma16(a0[s], bf[s], lo); hi = mfma16(a1[s], bf[s], hi); }
            if (role < 2) {
                u32x4 o = {pack2(lo.xy), pack2(lo.zw), pack2(hi.xy), pack2(hi.zw)};
                ST16(o, lane * 16, WS_MF + role * 294912 + ((4 * cb + wave) * 12 + s0) * 1024);
            } else {
                const unsigned u = pack2(lo.xy);
                const unsigned u0 = __shfl(u, 2 * sl), u1 = __shfl(u, 2 * sl + 1);
                const unsigned pay = lane < 32 ? ((u0 & 0xffffu) | (u1 << 16)) : ((u0 >> 16) | (u1 & 0xffff0000u));
                __hip_atomic_store((GAS u64*)(ws + WS_MODG + row8 * 4608 + (32 * slice + 8 * wave + sl) * 8), ((u64)(lane < 32 ? tag0 : tag1) << 32) | pay, RLX_AGENT);
            }
            __builtin_amdgcn_s_setprio(0);
            STAMP(3);
            wg_barrier();
            VMWAIT(1);
        }
    }
    if (!s0w) VMWAIT(0);
    asm volatile("" : "+v"(xq[0]), "+v"(xq[1]), "+v"(xq[2]), "+v"(xq[3]), "+v"(xq[4]), "+v"(xq[5]), "+v"(pf0));
    const unsigned vk = row8 * 768 + swz;
    const char* kp = (const char*)x + (b * 1024 + 128 * j + 64 * (wave >> 2)) * 768;
#define KEY_DMA() do { _Pragma("unroll 1") for (int rg = 0; rg < 8; ++rg) { \
        UNROLL for (int i = 0; i < 6; ++i) glds(vk, kp + rg * 6144 + i * 128, lds0 + L_XK + (((wave >> 2) * 8 + rg) * 6 + i) * 1024); } } while (0)
    if (wave == 3) KEY_DMA();
    f32x2 n[24];
    {
        float s0_ = 0.f, s1_ = 0.f, q0_ = 0.f, q1_ = 0.f;
        UNROLL for (int i = 0; i < 6; ++i) {
            s0_ = dot2(xq[i][0], 0x3f803f80u, s0_); q0_ = dot2(xq[i][0], xq[i][0], q0_); s1_ = dot2(xq[i][1], 0x3f803f80u, s1_); q1_ = dot2(xq[i][1], xq[i][1], q1_);
            s0_ = dot2(xq[i][2], 0x3f803f80u, s0_); q0_ = dot2(xq[i][2], xq[i][2], q0_); s1_ = dot2(xq[i][3], 0x3f803f80u, s1_); q1_ = dot2(xq[i][3], xq[i][3], q1_);
        }
        const f32x2 st = ln_coef(oct_sum(s0_ + s1_), oct_sum(q0_ + q1_));
        UNROLL for (int i = 0; i < 6; ++i)
            UNROLL for (int e = 0; e < 4; ++e) n[4 * i + e] = unpack2(xq[i][e]) * st[0] + st[1];
        UNROLL for (int i = 0; i < 24; ++i) asm volatile("" : "+v"(n[i]));
    }
    STAMP(4);
    if (wave < 4) {
        if (role < 2) {
            if (wave == 3) VMWAIT(48); else VMWAIT(0);
            const int ctg = 4 * cb + wave, jc = (ctg * 11) >> 5;
            if (lane < 32) put_flag(ws, WS_IN0 + (lane * 8 + jc) * 512 + role * 256 + ((ctg - 3 * jc) * 12 + s0) * 4, lane < 16 ? tag0 : tag1);
        }
    } else {
        if (role < 3) wg_barrier();
        if (wave == 7) KEY_DMA();
        else {
            GAS u64* gp = (GAS u64*)(ws + WS_MODG + xg * 4608) + w4 * 192 + lane;
            u64 g0, g1, g2;
#pragma unroll 1
            for (int spin = 0; spin < SPIN_CAP; ++spin) {
                g0 = __hip_atomic_load(gp, RLX_AGENT); g1 = __hip_atomic_load(gp + 64, RLX_AGENT); g2 = __hip_atomic_load(gp + 128, RLX_AGENT);
                if (__all((unsigned)(g0 >> 32) == tag && (unsigned)(g1 >> 32) == tag && (unsigned)(g2 >> 32) == tag)) break;
                __builtin_amdgcn_s_sleep(1);
            }
            const float add = w4 == 1 ? 1.f : 0.f;
            LAS char* pm = lds + L_MOD + w4 * 1536 + lane * 8;
            *(LAS f32x2*)(pm) = unpack2((unsigned)g0) + add;
            *(LAS f32x2*)(pm + 512) = unpack2((unsigned)g1) + add;
            *(LAS f32x2*)(pm + 1024) = unpack2((unsigned)g2) + add;
        }
    }
    STAMP(5);
    wg_barrier();
    STAMP(6);
    const unsigned f0off = WS_IN0 + cid * 512 + (wave < 4 ? 256 : 0) + w4 * 48 + min(lane, 11) * 4;
    unsigned f0 = tag;
    if (w4 < 3) f0 = __hip_atomic_load((GAS unsigned*)(ws + f0off), RLX_AGENT);
    SB();
    {
        LAS char* pm = opaque(lds + L_MOD + sl * 32);
        LAS char* ph = opaque(lds + L_H + wave * 6144 + row8 * 128 + swz);
        UNROLL for (int i = 0; i < 6; ++i) {
            const f32x4 t0 = F4(pm, i * 256), t1 = F4(pm, i * 256 + 16), c0 = F4(pm, i * 256 + 1536), c1 = F4(pm, i * 256 + 1536 + 16);
            u32x4 o;
            o[0] = pack2(n[4 * i + 0] * c0.xy + t0.xy);
            o[1] = pack2(n[4 * i + 1] * c0.zw + t0.zw);
            o[2] = pack2(n[4 * i + 2] * c1.xy + t1.xy);
            o[3] = pack2(n[4 * i + 3] * c1.zw + t1.zw);
            U4(ph, i * 1024) = o;
        }
    }
    STAMP(7);

    const int ct = 3 * j + w4;
#define STAGE1_GEMM(MBASE, MFMA_EXPR)                                                                  \
        if (!__all(f0 == tag)) poll_flags(ws, f0off, tag);                                             \
        asm volatile("" ::: "memory");                                                                 \
        STAMP(8);                                                                                      \
        u32x4 mf[12], hf[12][4];                                                                       \
        UNROLL for (int s = 0; s < 12; ++s) mf[s] = LD16(lane * 16, (MBASE) + s * 1024);               \
        SB();                                                                                          \
        wg_barrier();                                                          \
        LAS char* pe = opaque(lds + L_H + lpe); LAS char* po = opaque(lds + L_H + lpo);                \
        UNROLL for (int s = 0; s < 8; ++s)                                                             \
            UNROLL for (int tt = 0; tt < 4; ++tt) hf[s][tt] = FRAG(pe, po, tt, s);                     \
        SB();                                                                                          \
        f32x4 ac[4] = {};                                                                              \
        UNROLL for (int s = 0; s < 12; ++s) {                                                          \
            UNROLL for (int tt = 0; tt < 4; ++tt) {                                                    \
                ac[tt] = MFMA_EXPR;                                                                    \
                if (s + 8 < 12) hf[s + 8][tt] = FRAG(pe, po, tt, s + 8);                               \
                SB();                                                                                  \
            }                                                                                          \
        }
    if (w4 == 3) {
        wg_barrier();
        VMWAIT(0);
        STAMP(8);
        const u32x4 ones = {0x3f803f80u, 0x3f803f80u, 0x3f803f80u, 0x3f803f80u};
#pragma unroll 1
        for (int t = 0; t < 4; ++t) {
            const int tile = (wave >> 2) * 4 + t;
            LAS char* pe = lds + L_XK + tile * 12288 + lpe; LAS char* po = lds + L_XK + tile * 12288 + lpo;
            u32x4 f[12];
            UNROLL for (int s = 0; s < 12; ++s) f[s] = FRAG(pe, po, 0, s);
            SB();
            f32x4 sm = {}, gr = {};
            UNROLL for (int s = 0; s < 12; ++s) { sm = mfma16(ones, f[s], sm); gr = mfma16(f[s], f[s], gr); }
            const float d = (r & 2) ? ((r & 1) ? gr[3] : gr[2]) : ((r & 1) ? gr[1] : gr[0]);
            const f32x2 st = ln_coef(sm[0], d);
            if (g == (r >> 2)) { *(LAS float*)(lds + L_KST + (tile * 16 + r) * 4) = st[0]; *(LAS float*)(lds + L_KST + 512 + (tile * 16 + r) * 4) = st[1]; }
        }
        STAMP(9);
    } else if (wave > 3) {
        __builtin_amdgcn_s_setprio(2);
        const f32x4 qs = F4(lds, L_MOD + 1536 + (16 * ct + 4 * g) * 4) * (1.4426950408889634f * 0.05103103630798288f);
        STAGE1_GEMM(WS_MF + ct * 12288, mfma16(mf[s], hf[s][tt], ac[tt]))
        const unsigned vo = ((2 * (ct & 1) + (g >> 1)) * 16 + r) * 16 + (g & 1) * 8;
        UNROLL for (int tt = 0; tt < 4; ++tt) {
            const f32x4 v = ac[tt] * qs;
            u32x2 o = {pack2(v.xy), pack2(v.zw)};
            ST8(o, vo, WS_QF + ((4 * qbg + tt) * 12 + (ct >> 1)) * 1024);
        }
        __builtin_amdgcn_s_setprio(0);
        STAMP(9);
        VMWAIT(0);
        if (lane < 8) put_flag(ws, WS_IN1Q + (qbg * 8 + lane) * 128 + ct * 4, tag);
    } else {
        STAGE1_GEMM(WS_MF + 294912 + ct * 12288, mfma16(hf[s][tt], mf[s], ac[tt]))
        const int vw = j >> 1, vc = ct - vw * 6;
        const unsigned vo = (g >> 1) * 1024 + ((g & 1) * 32 + 16 * (ct & 1) + r) * 16;
        UNROLL for (int tp = 0; tp < 2; ++tp) {
            u32x4 o = {pack2(ac[2 * tp].xy), pack2(ac[2 * tp].zw), pack2(ac[2 * tp + 1].xy), pack2(ac[2 * tp + 1].zw)};
            ST16(o, vo, WS_VF + ((((qbg >> 1) * 4 + vw) * 3 + (vc >> 1)) * 8 + 4 * (qbg & 1) + 2 * tp) * 1024);
        }
        STAMP(9);
        VMWAIT(0);
        if (lane < 32) put_flag(ws, WS_IN1V + ((b * 16 + (lane >> 1)) * 8 + 2 * vw + (lane & 1)) * 512 + ((qbg >> 1) & 7) * 64 + ((qbg & 1) * 6 + vc) * 4, tag);
    }
    STAMP(10);
    wg_barrier();
    STAMP(11);

    if (wave > 3) {
        LAS char* pe0 = opaque(lds + L_XK + lpe); LAS char* po0 = opaque(lds + L_XK + lpo);
        LAS char* pe1 = opaque(lds + L_XK + 49152 + lpe); LAS char* po1 = opaque(lds + L_XK + 49152 + lpo);
        u32x4 kf[12][8], qf[12];
        f32x4 krs[8], kmu[8];
#define KF(s, t) ((t) < 4 ? FRAG(pe0, po0, (t), (s)) : FRAG(pe1, po1, (t) - 4, (s)))
        UNROLL for (int s = 0; s < 4; ++s)
            UNROLL for (int t = 0; t < 8; ++t) kf[s][t] = KF(s, t);
        LAS char* pst = opaque(lds + L_KST + g * 16);
        SB();
        poll_flags(ws, WS_IN1Q + cid * 128 + min(lane, 23) * 4, tag);
        __builtin_amdgcn_s_setprio(2);
        STAMP(12);
        UNROLL for (int s = 0; s < 12; ++s) qf[s] = LD16(lane * 16, WS_QF + ((4 * qbg + w4) * 12 + s) * 1024);
        SB();
        f32x4 sa[8] = {};
        float rq0 = 0.f, rq1 = 0.f;
        UNROLL for (int s = 0; s < 12; ++s) {
            UNROLL for (int t = 0; t < 8; ++t) {
                sa[t] = mfma16(kf[s][t], qf[s], sa[t]);
                if (s + 4 < 12) kf[s + 4][t] = KF(s + 4, t);
                else if (s == 9) krs[t] = F4(pst, t * 64);
                else if (s == 10) kmu[t] = F4(pst, 512 + t * 64);
                if (t == 0) rq0 = dot2(qf[s][0], 0x3f803f80u, rq0);
                if (t == 1) rq1 = dot2(qf[s][1], 0x3f803f80u, rq1);
                if (t == 2) rq0 = dot2(qf[s][2], 0x3f803f80u, rq0);
                if (t == 3) rq1 = dot2(qf[s][3], 0x3f803f80u, rq1);
                if (s == 11 && t == 4) rq0 = rows_sum(rq0 + rq1);
                SB();
            }
        }
        STAMP(13);
        float mx = -3.0e38f;
        UNROLL for (int t = 0; t < 8; ++t) {
            sa[t] = sa[t] * krs[t] + kmu[t] * rq0;
            mx = fmaxf(fmaxf(fmaxf(mx, sa[t][0]), sa[t][1]), fmaxf(sa[t][2], sa[t][3]));
        }
        mx = rows_max(mx);
        f32x4 ls = {};
        const unsigned vo = (g >> 1) * 1024 + ((g & 1) * 32 + (w4 & 1) * 16 + r) * 16;
        UNROLL for (int tp = 0; tp < 4; ++tp) {
            UNROLL for (int t = 2 * tp; t < 2 * tp + 2; ++t) {
                sa[t] -= mx;
                UNROLL for (int e = 0; e < 4; ++e) sa[t][e] = __builtin_amdgcn_exp2f(sa[t][e]);
                ls += sa[t];
            }
            u32x4 o = {pack2(sa[2 * tp].xy), pack2(sa[2 * tp].zw), pack2(sa[2 * tp + 1].xy), pack2(sa[2 * tp + 1].zw)};
            ST16(o, vo, WS_PF + ((cid * 2 + (w4 >> 1)) * 8 + 2 * tp) * 1024);
        }
        const float l = rows_sum((ls[0] + ls[1]) + (ls[2] + ls[3]));
        const u32x2 ml = {__float_as_uint(mx), __float_as_uint(l)};
        if (g == 0) ST8(ml, r * 8, WS_ML + cid * 512 + w4 * 128);
        __builtin_amdgcn_s_setprio(0);
        STAMP(14);
        wg_barrier();
    } else {
        poll_flags(ws, WS_IN1V + cid * 512 + (wave + 4 * (lane >> 5)) * 64 + min(lane & 31, 11) * 4, tag);
        STAMP(12);
    }

    u32x4 vf[24];
    UNROLL for (int i = 0; i < 24; ++i) vf[i] = LD16(lane * 16, WS_VF + (((b * 8 + wave) * 4 + (j >> 1)) * 24 + i) * 1024);
    SB();
    if (wave > 3) {
        VMWAIT(24);
        if (lane < 4) put_flag(ws, WS_IN2 + (qbg * 8 + 2 * lane + (w4 >> 1)) * 128 + (j * 2 + (w4 & 1)) * 4, tag);
        STAMP(15);
    } else wg_barrier();
    const int oq = (tid * 43691) >> 19, oc = tid - 12 * oq;
    const int ooff = (64 * qbg + 32 * (j & 1) + oq) * D + 96 * (j >> 1) + 8 * oc;
    u32x4 xr = {};
    if (wave < 6) xr = *(const u32x4*)(x + ooff);
    poll_flags(ws, WS_IN2 + cid * 128 + (wave * 2 + (lane & 1)) * 4, tag);
    STAMP(16);
    {
        u32x4 pf[8];
        const int prod = qbg * 8 + wave;
        UNROLL for (int ks = 0; ks < 8; ++ks) pf[ks] = LD16(lane * 16, WS_PF + ((prod * 2 + (j & 1)) * 8 + ks) * 1024);
        const u32x2 ml = __builtin_amdgcn_raw_buffer_load_b64(rs, (lane & 31) * 8, WS_ML + prod * 512 + (j & 1) * 256, 16);
        SB();
        LAS char* pw = lds + L_XK + (wave * 32 + (lane & 31)) * PROW + (lane >> 5) * 16;
        f32x16 oa[3];
        UNROLL for (int dt = 0; dt < 4; ++dt) {
            if (dt < 3) {
                oa[dt] = (f32x16){};
                UNROLL for (int ks = 0; ks < 8; ++ks) oa[dt] = mfma32(vf[dt * 8 + ks], pf[ks], oa[dt]);
            }
            if (dt > 0) {
                const f32x16& a = oa[dt - 1];
                UNROLL for (int i = 0; i < 2; ++i) {
                    auto r0 = __builtin_amdgcn_permlane32_swap(pack2(a[8 * i], a[8 * i + 1]), pack2(a[8 * i + 4], a[8 * i + 5]), false, false);
                    auto r1 = __builtin_amdgcn_permlane32_swap(pack2(a[8 * i + 2], a[8 * i + 3]), pack2(a[8 * i + 6], a[8 * i + 7]), false, false);
                    U4(pw, (dt - 1) * 64 + i * 32) = (u32x4){r0[0], r1[0], r0[1], r1[1]};
                }
            }
            if (dt == 1) { if (lane < 32) *(LAS u32x2*)(lds + L_ML + (wave * 32 + lane) * 8) = ml; }
            if (dt > 0 && dt < 3) {
                __builtin_amdgcn_sched_group_barrier(0x008, 2, 0);
                UNROLL for (int k = 0; k < 6; ++k) { __builtin_amdgcn_sched_group_barrier(0x282, 3, 0); __builtin_amdgcn_sched_group_barrier(0x008, 1, 0); }
            }
            SB();
        }
    }
    STAMP(17);
    wg_barrier();
    STAMP(18);
    if (wave < 6) {
        f32x2 ml[8]; u32x4 pv[8];
        UNROLL for (int k = 0; k < 8; ++k) ml[k] = *(LAS f32x2*)(lds + L_ML + (k * 32 + oq) * 8);
        UNROLL for (int k = 0; k < 8; ++k) pv[k] = U4(lds, L_XK + (k * 32 + oq) * PROW + oc * 16);
        const f32x4 gt0 = F4(lds, L_MOD + 3072 + (96 * (j >> 1) + 8 * oc) * 4), gt1 = F4(lds, L_MOD + 3072 + (96 * (j >> 1) + 8 * oc) * 4 + 16);
        SB();
        float m = ml[0][0];
        UNROLL for (int k = 1; k < 8; ++k) m = fmaxf(m, ml[k][0]);
        float wk[8], L = 0.f;
        UNROLL for (int k = 0; k < 8; ++k) { wk[k] = __builtin_amdgcn_exp2f(ml[k][0] - m); L = fmaf(wk[k], ml[k][1], L); }
        const float inv = __builtin_amdgcn_rcpf(L);
        f32x2 o[4] = {};
        UNROLL for (int k = 0; k < 8; ++k)
            UNROLL for (int e = 0; e < 4; ++e) o[e] += unpack2(pv[k][e]) * wk[k];
        u32x4 w = {pack2(o[0] * inv * gt0.xy + unpack2(xr[0])), pack2(o[1] * inv * gt0.zw + unpack2(xr[1])),
                   pack2(o[2] * inv * gt1.xy + unpack2(xr[2])), pack2(o[3] * inv * gt1.zw + unpack2(xr[3]))};
        *(u32x4*)(out + ooff) = w;
        if ((cid & 127) == 0 && tid == 0) put_flag(ws, WS_EPOCH + b * 4, tag);
    }
    STAMP(29); STAMPC(31);
    STAMP_OUT;
    asm volatile(".Lpf_end:");
}

extern "C" void kernel_launch(void* const* d_in, const int* in_sizes, int n_in, void* d_out, int out_size, void* d_ws, size_t ws_size, hipStream_t stream) {
    dit_fused<<<256, 512, 0, stream>>>((const u16*)d_in[0], (const u16*)d_in[1], (const u16*)d_in[2], (const u16*)d_in[3], (const u16*)d_in[4], (u16*)d_out, (char*)d_ws);
}
```

```cpp
#define FIX 1
#include <hip/hip_runtime.h>
#include <hip/hip_bf16.h>

typedef unsigned short u16;
typedef unsigned long long u64;
typedef __bf16 bf16x2 __attribute__((ext_vector_type(2)));
typedef __bf16 bf16x8 __attribute__((ext_vector_type(8)));
typedef short s16x4 __attribute__((ext_vector_type(4)));
typedef float f32x2 __attribute__((ext_vector_type(2)));
typedef float f32x4 __attribute__((ext_vector_type(4)));
typedef float f32x16 __attribute__((ext_vector_type(16)));
typedef unsigned u32x2 __attribute__((ext_vector_type(2)));
typedef unsigned u32x4 __attribute__((ext_vector_type(4)));
#define GAS __attribute__((address_space(1)))
#define LAS __attribute__((address_space(3)))
#define DEV __device__ __forceinline__
#define RLX_AGENT __ATOMIC_RELAXED, __HIP_MEMORY_SCOPE_AGENT
#define SB() __builtin_amdgcn_sched_barrier(0)
#define UNROLL _Pragma("unroll")

constexpr int D = 384;
constexpr unsigned WS_EPOCH = 0;
constexpr unsigned WS_MODG = 256;
constexpr unsigned WS_IN0 = WS_MODG + 8 * 4608;
constexpr unsigned WS_IN1Q = WS_IN0 + 256 * 512;
constexpr unsigned WS_IN1V = WS_IN1Q + 256 * 128;
constexpr unsigned WS_IN2 = WS_IN1V + 256 * 512;
constexpr unsigned WS_MF = WS_IN2 + 256 * 128;
constexpr unsigned WS_QF = WS_MF + 2 * 294912;
constexpr unsigned WS_VF = WS_QF + 1572864;
constexpr unsigned WS_PF = WS_VF + 1572864;
constexpr unsigned WS_ML = WS_PF + 256 * 16384;
constexpr unsigned WS_END = WS_ML + 256 * 512;
constexpr unsigned WS_STAMP = WS_END;
constexpr int L_XK = 0;
constexpr int L_SA = 0, L_SB = 24576;
constexpr int L_H = 98304;
constexpr int L_MOD = 147456;
constexpr int L_KST = 152064;
constexpr int L_ML = 153088;
constexpr int L_TOTAL = 155136;
constexpr int PROW = 208;
constexpr int SPIN_CAP = 1 << 16;
constexpr int NWAVES = 8;

#define STAMP(i) do { if ((i) == 0) { SB(); amp0 = (unsigned)__builtin_amdgcn_s_memrealtime(); SB(); } } while (0)
#define STAMPC(i) do {} while (0)
#define STAMP_DECL unsigned amp0 = 0
#define STAMP_OUT do { while ((unsigned)__builtin_amdgcn_s_memrealtime() - amp0 < 4000u) __builtin_amdgcn_s_sleep(2); } while (0)

DEV void wg_barrier() { asm volatile("s_waitcnt lgkmcnt(0)\n\ts_barrier" ::: "memory"); }
#define VMWAIT(n) asm volatile("s_waitcnt vmcnt(" #n ")" ::: "memory")
DEV unsigned pack2(float a, float b) { f32x2 f = {a, b}; return __builtin_bit_cast(unsigned, __builtin_convertvector(f, bf16x2)); }
DEV unsigned pack2(f32x2 f) { return __builtin_bit_cast(unsigned, __builtin_convertvector(f, bf16x2)); }
DEV float blo(unsigned u) { return __uint_as_float(u << 16); }
DEV float bhi(unsigned u) { return __uint_as_float(u & 0xffff0000u); }
DEV f32x2 unpack2(unsigned u) { return (f32x2){blo(u), bhi(u)}; }
DEV float dot2(unsigned a, unsigned b, float c) { return __builtin_amdgcn_fdot2_f32_bf16(__builtin_bit_cast(bf16x2, a), __builtin_bit_cast(bf16x2, b), c, false); }
DEV f32x4 mfma16(u32x4 a, u32x4 b, f32x4 c) { return __builtin_amdgcn_mfma_f32_16x16x32_bf16(__builtin_bit_cast(bf16x8, a), __builtin_bit_cast(bf16x8, b), c, 0, 0, 0); }
DEV f32x16 mfma32(u32x4 a, u32x4 b, f32x16 c) { return __builtin_amdgcn_mfma_f32_32x32x16_bf16(__builtin_bit_cast(bf16x8, a), __builtin_bit_cast(bf16x8, b), c, 0, 0, 0); }
DEV float rows_sum(float v) {
    auto a = __builtin_amdgcn_permlane16_swap(__float_as_uint(v), __float_as_uint(v), false, false);
    v = __uint_as_float(a[0]) + __uint_as_float(a[1]);
    auto b = __builtin_amdgcn_permlane32_swap(__float_as_uint(v), __float_as_uint(v), false, false);
    return __uint_as_float(b[0]) + __uint_as_float(b[1]);
}
DEV float rows_max(float v) {
    auto a = __builtin_amdgcn_permlane16_swap(__float_as_uint(v), __float_as_uint(v), false, false);
    v = fmaxf(__uint_as_float(a[0]), __uint_as_float(a[1]));
    auto b = __builtin_amdgcn_permlane32_swap(__float_as_uint(v), __float_as_uint(v), false, false);
    return fmaxf(__uint_as_float(b[0]), __uint_as_float(b[1]));
}
DEV float oct_sum(float v) {
    v += __int_as_float(__builtin_amdgcn_mov_dpp(__float_as_int(v), 0xB1, 0xf, 0xf, true));
    v += __int_as_float(__builtin_amdgcn_mov_dpp(__float_as_int(v), 0x4E, 0xf, 0xf, true));
    v += __int_as_float(__builtin_amdgcn_mov_dpp(__float_as_int(v), 0x141, 0xf, 0xf, true));
    return v;
}
DEV f32x2 ln_coef(float sum, float sq) {
    const float mu = sum * (1.f / D);
    const float rstd = __builtin_amdgcn_rsqf(fmaxf(sq * (1.f / D) - mu * mu, 0.f) + 1e-5f);
    return (f32x2){rstd, -mu * rstd};
}
DEV void poll_flags(char* ws, unsigned off, unsigned tag) {
    GAS unsigned* p = (GAS unsigned*)(ws + off);
#pragma unroll 1
    for (int spin = 0; spin < SPIN_CAP; ++spin) {
        unsigned v = __hip_atomic_load(p, RLX_AGENT);
        if (__all(v == tag)) break;
        __builtin_amdgcn_s_sleep(1);
    }
    asm volatile("" ::: "memory");
}
DEV void put_flag(char* ws, unsigned off, unsigned tag) { __hip_atomic_store((GAS unsigned*)(ws + off), tag, RLX_AGENT); }
DEV void glds(unsigned voff, const char* sbase, unsigned ldsdst) {
    asm volatile("s_mov_b32 m0, %2\n\ts_nop 0\n\tglobal_load_lds_dwordx4 %0, %1" :: "v"(voff), "s"(sbase), "s"(ldsdst) : "memory");
}
#define U4(p, off) (*(LAS u32x4*)((p) + (off)))
#define F4(p, off) (*(LAS f32x4*)((p) + (off)))
#define LD16(voff, soff) __builtin_amdgcn_raw_buffer_load_b128(rs, (voff), (soff), 16)
#define ST16(v, voff, soff) __builtin_amdgcn_raw_buffer_store_b128((v), rs, (voff), (soff), 16)
#define ST8(v, voff, soff) __builtin_amdgcn_raw_buffer_store_b64((v), rs, (voff), (soff), 16)
DEV LAS char* opaque(LAS char* p) { asm volatile("" : "+v"(p)); return p; }
#define FRAG(pe, po, t, s) U4(((s) & 1) ? (po) : (pe), ((t) * 12 + ((s) >> 1)) * 1024)

extern "C" __global__ __launch_bounds__(512, 2) void dit_fused(const u16* __restrict__ x, const u16* __restrict__ cvec, const u16* __restrict__ wqkv,
                                                               const u16* __restrict__ wo, const u16* __restrict__ wmod, u16* __restrict__ out, char* ws) {
    __shared__ __attribute__((aligned(16))) char smem[L_TOTAL];
    LAS char* lds = (LAS char*)smem;
    const unsigned lds0 = (unsigned)(size_t)lds;
    const int tid = threadIdx.x, lane = tid & 63;
    const int wave = __builtin_amdgcn_readfirstlane(tid >> 6);
    const int bid = blockIdx.x, xg = bid & 7, idx = bid >> 3;
    STAMP_DECL;
    STAMP(0); STAMPC(30);
    unsigned pf0;
    {
        u64 pc; unsigned len;
        asm volatile("s_getpc_b64 %0\n.Lpf_here:\n\ts_mov_b32 %1, .Lpf_end-.Lpf_here" : "=s"(pc), "=s"(len));
        unsigned o0 = min((unsigned)(((idx & 3) * 8 + wave) * 8 + (lane >> 3)) * 128u, len);
        asm volatile("global_load_dword %0, %1, %2" : "=&v"(pf0) : "v"(o0), "s"(pc) : "memory");
    }
    asm volatile("" :: "s"(x), "s"(cvec), "s"(wqkv), "s"(wo), "s"(wmod), "s"(out), "s"(ws));
    u64 ep;
    asm volatile("s_load_dwordx2 %0, %1, 0x0" : "=s"(ep) : "s"(ws) : "memory");

    const int b = xg >> 2;
    const int qbg = b * 16 + (xg & 3) * 4 + (idx >> 3);
    const int j = idx & 7;
    const int cid = qbg * 8 + j;
    const int r = lane & 15, g = lane >> 4, row8 = lane >> 3, sl = lane & 7;
    const unsigned swz = (unsigned)(sl ^ row8) * 16u;
    const int slice = xg + 8 * (idx - 18);
    const int role = idx < 9 ? 0 : idx < 18 ? 1 : slice < 18 ? 2 : 3;
    const int w4 = wave & 3;
    const int ti = idx - (role == 1 ? 9 : 0);
    const int t3 = (ti * 11) >> 5;
    const int s0 = 3 * (xg >> 1) + t3, cb = 3 * (xg & 1) + (ti - 3 * t3);

    unsigned craw = 0;
    if (role < 3) {
        if (role == 2) {
            asm volatile("global_load_dword %0, %1, %2" : "=v"(craw) : "v"(min(tid, 383) * 4), "s"(cvec) : "memory");
        } else {
            const int rg = wave >> 1, c0 = 3 * (wave & 1);
            const char* ap = (const char*)wqkv + (32 * s0 + 16 * (rg & 1) + 4 * (rg >> 1)) * 2304 + role * 1536 + c0 * 128;
            const unsigned va = (8 * (row8 >> 2) + (row8 & 3)) * 2304 + swz;
            UNROLL for (int i = 0; i < 3; ++i) glds(va, ap + i * 128, lds0 + L_SA + (rg * 6 + c0 + i) * 1024);
        }
        const int ldb = role == 0 ? 2304 : role == 1 ? 768 : 4608;
        const char* bp = role == 0 ? (const char*)wqkv + 768 + (64 * cb + 8 * wave) * 2304
                       : (role == 1 ? (const char*)wo + cb * 128 : (const char*)wmod + slice * 128) + 48 * wave * ldb;
        const int stepb = role == 0 ? 128 : 8 * ldb;
        const unsigned vb = row8 * ldb + swz;
        UNROLL for (int i = 0; i < 6; ++i) glds(vb, bp + i * stepb, lds0 + L_SB + (wave * 6 + i) * 1024);
    }
    u32x4 xq[6];
    {
        const char* xp = (const char*)x + (64 * qbg + 8 * wave) * 768;
        const unsigned vx = row8 * 768 + sl * 16;
        asm volatile("global_load_dwordx4 %0, %6, %7\n\tglobal_load_dwordx4 %1, %6, %7 offset:128\n\tglobal_load_dwordx4 %2, %6, %7 offset:256\n\t"
                     "global_load_dwordx4 %3, %6, %7 offset:384\n\tglobal_load_dwordx4 %4, %6, %7 offset:512\n\tglobal_load_dwordx4 %5, %6, %7 offset:640"
                     : "=&v"(xq[0]), "=&v"(xq[1]), "=&v"(xq[2]), "=&v"(xq[3]), "=&v"(xq[4]), "=&v"(xq[5]) : "v"(vx), "s"(xp) : "memory");
    }
    STAMP(1);
    const __amdgpu_buffer_rsrc_t rs = __builtin_amdgcn_make_buffer_rsrc(ws, 0, WS_END, 0x00020000);
    const int lpe = (r >> 3) * 6144 + (r & 7) * 128 + ((g ^ (r & 7)) * 16), lpo = lpe ^ 64;
    if (role == 2) {
        asm volatile("s_waitcnt vmcnt(12)" : "+v"(craw) :: "memory");
        float a = blo(craw), c = bhi(craw);
        a = a * __builtin_amdgcn_rcpf(1.f + __builtin_amdgcn_exp2f(-1.4426950408889634f * a));
        c = c * __builtin_amdgcn_rcpf(1.f + __builtin_amdgcn_exp2f(-1.4426950408889634f * c));
        const int row = tid >= 192, dw = tid - 192 * row, ch = dw >> 2;
        if (tid < 384) *(LAS unsigned*)(lds + L_SA + (ch >> 3) * 1024 + row * 128 + (((ch & 7) ^ row) * 16) + (dw & 3) * 4) = pack2(a, c);
    }
    u64 e_;
    asm volatile("s_waitcnt lgkmcnt(0)\n\ts_mov_b64 %0, %1" : "=s"(e_) : "s"(ep));
    const unsigned tag0 = (unsigned)e_ + 1u, tag1 = (unsigned)(e_ >> 32) + 1u, tag = b ? tag1 : tag0;

    const bool s0w = role < 3 && wave < 4;
    if (role < 3) {
        VMWAIT(6);
        wg_barrier();
        STAMP(2);
        if (wave < 4) {
            __builtin_amdgcn_s_setprio(2);
            u32x4 a0[12], a1[12], bf[12];
            LAS char* pe = opaque(lds + lpe); LAS char* po = opaque(lds + lpo);
            if (role == 0) {
                LAS char* qe = pe + L_SB + wave * 12288; LAS char* qo = po + L_SB + wave * 12288;
                UNROLL for (int s = 0; s < 12; ++s) { bf[s] = FRAG(qe, qo, 0, s); a0[s] = FRAG(pe, po, 0, s); a1[s] = FRAG(pe, po, 1, s); }
            } else {
                const int q = (lane >> 2) & 3, p = lane & 3;
                LAS char* t0 = opaque(lds + L_SB + g * 1024 + q * 128 + (((2 * wave + (p >> 1)) ^ q) * 16) + 8 * (p & 1));
                LAS char* t1 = opaque(lds + L_SB + g * 1024 + (4 + q) * 128 + (((2 * wave + (p >> 1)) ^ (4 + q)) * 16) + 8 * (p & 1));
                UNROLL for (int s = 0; s < 12; ++s) {
                    u32x2 l2 = __builtin_bit_cast(u32x2, __builtin_amdgcn_ds_read_tr16_b64_v4i16((LAS s16x4*)(t0 + s * 4096)));
                    u32x2 h2 = __builtin_bit_cast(u32x2, __builtin_amdgcn_ds_read_tr16_b64_v4i16((LAS s16x4*)(t1 + s * 4096)));
                    bf[s] = (u32x4){l2[0], l2[1], h2[0], h2[1]};
                    a0[s] = FRAG(pe, po, 0, s); a1[s] = FRAG(pe, po, 1, s);
                }
            }
            SB();
            f32x4 lo = {}, hi = {};
            UNROLL for (int s = 0; s < 12; ++s) { lo = mfma16(a0[s], bf[s], lo); hi = mfma16(a1[s], bf[s], hi); }
            if (role < 2) {
                u32x4 o = {pack2(lo.xy), pack2(lo.zw), pack2(hi.xy), pack2(hi.zw)};
                ST16(o, lane * 16, WS_MF + role * 294912 + ((4 * cb + wave) * 12 + s0) * 1024);
            } else {
                const unsigned u = pack2(lo.xy);
                const unsigned u0 = __shfl(u, 2 * sl), u1 = __shfl(u, 2 * sl + 1);
                const unsigned pay = lane < 32 ? ((u0 & 0xffffu) | (u1 << 16)) : ((u0 >> 16) | (u1 & 0xffff0000u));
                __hip_atomic_store((GAS u64*)(ws + WS_MODG + row8 * 4608 + (32 * slice + 8 * wave + sl) * 8), ((u64)(lane < 32 ? tag0 : tag1) << 32) | pay, RLX_AGENT);
            }
            __builtin_amdgcn_s_setprio(0);
            STAMP(3);
            wg_barrier();
            VMWAIT(1);
        }
    }
    if (!s0w) VMWAIT(0);
    asm volatile("" : "+v"(xq[0]), "+v"(xq[1]), "+v"(xq[2]), "+v"(xq[3]), "+v"(xq[4]), "+v"(xq[5]), "+v"(pf0));
    const unsigned vk = row8 * 768 + swz;
    const char* kp = (const char*)x + (b * 1024 + 128 * j + 64 * (wave >> 2)) * 768;
#define KEY_DMA() do { _Pragma("unroll 1") for (int rg = 0; rg < 8; ++rg) { \
        UNROLL for (int i = 0; i < 6; ++i) glds(vk, kp + rg * 6144 + i * 128, lds0 + L_XK + (((wave >> 2) * 8 + rg) * 6 + i) * 1024); } } while (0)
    if (wave == 3) KEY_DMA();
    f32x2 n[24];
    {
        float s0_ = 0.f, s1_ = 0.f, q0_ = 0.f, q1_ = 0.f;
        UNROLL for (int i = 0; i < 6; ++i) {
            s0_ = dot2(xq[i][0], 0x3f803f80u, s0_); q0_ = dot2(xq[i][0], xq[i][0], q0_); s1_ = dot2(xq[i][1], 0x3f803f80u, s1_); q1_ = dot2(xq[i][1], xq[i][1], q1_);
            s0_ = dot2(xq[i][2], 0x3f803f80u, s0_); q0_ = dot2(xq[i][2], xq[i][2], q0_); s1_ = dot2(xq[i][3], 0x3f803f80u, s1_); q1_ = dot2(xq[i][3], xq[i][3], q1_);
        }
        const f32x2 st = ln_coef(oct_sum(s0_ + s1_), oct_sum(q0_ + q1_));
        UNROLL for (int i = 0; i < 6; ++i)
            UNROLL for (int e = 0; e < 4; ++e) n[4 * i + e] = unpack2(xq[i][e]) * st[0] + st[1];
        UNROLL for (int i = 0; i < 24; ++i) asm volatile("" : "+v"(n[i]));
    }
    STAMP(4);
    if (wave < 4) {
        if (role < 2) {
            if (wave == 3) VMWAIT(48); else VMWAIT(0);
            const int ctg = 4 * cb + wave, jc = (ctg * 11) >> 5;
            if (lane < 32) put_flag(ws, WS_IN0 + (lane * 8 + jc) * 512 + role * 256 + ((ctg - 3 * jc) * 12 + s0) * 4, lane < 16 ? tag0 : tag1);
        }
    } else {
        if (role < 3) wg_barrier();
        if (wave == 7) KEY_DMA();
        else {
            GAS u64* gp = (GAS u64*)(ws + WS_MODG + xg * 4608) + w4 * 192 + lane;
            u64 g0, g1, g2;
#pragma unroll 1
            for (int spin = 0; spin < SPIN_CAP; ++spin) {
                g0 = __hip_atomic_load(gp, RLX_AGENT); g1 = __hip_atomic_load(gp + 64, RLX_AGENT); g2 = __hip_atomic_load(gp + 128, RLX_AGENT);
                if (__all((unsigned)(g0 >> 32) == tag && (unsigned)(g1 >> 32) == tag && (unsigned)(g2 >> 32) == tag)) break;
                __builtin_amdgcn_s_sleep(1);
            }
            const float add = w4 == 1 ? 1.f : 0.f;
            LAS char* pm = lds + L_MOD + w4 * 1536 + lane * 8;
            *(LAS f32x2*)(pm) = unpack2((unsigned)g0) + add;
            *(LAS f32x2*)(pm + 512) = unpack2((unsigned)g1) + add;
            *(LAS f32x2*)(pm + 1024) = unpack2((unsigned)g2) + add;
        }
    }
    STAMP(5);
    wg_barrier();
    STAMP(6);
    const unsigned f0off = WS_IN0 + cid * 512 + (wave < 4 ? 256 : 0) + w4 * 48 + min(lane, 11) * 4;
    unsigned f0 = tag;
    if (w4 < 3) f0 = __hip_atomic_load((GAS unsigned*)(ws + f0off), RLX_AGENT);
    SB();
    {
        LAS char* pm = opaque(lds + L_MOD + sl * 32);
        LAS char* ph = opaque(lds + L_H + wave * 6144 + row8 * 128 + swz);
        UNROLL for (int i = 0; i < 6; ++i) {
            const f32x4 t0 = F4(pm, i * 256), t1 = F4(pm, i * 256 + 16), c0 = F4(pm, i * 256 + 1536), c1 = F4(pm, i * 256 + 1536 + 16);
            u32x4 o;
            o[0] = pack2(n[4 * i + 0] * c0.xy + t0.xy);
            o[1] = pack2(n[4 * i + 1] * c0.zw + t0.zw);
            o[2] = pack2(n[4 * i + 2] * c1.xy + t1.xy);
            o[3] = pack2(n[4 * i + 3] * c1.zw + t1.zw);
            U4(ph, i * 1024) = o;
        }
    }
    STAMP(7);

    const int ct = 3 * j + w4;
#define STAGE1_GEMM(MBASE, MFMA_EXPR)                                                                  \
        if (!__all(f0 == tag)) poll_flags(ws, f0off, tag);                                             \
        asm volatile("" ::: "memory");                                                                 \
        STAMP(8);                                                                                      \
        u32x4 mf[12], hf[12][4];                                                                       \
        UNROLL for (int s = 0; s < 12; ++s) mf[s] = LD16(lane * 16, (MBASE) + s * 1024);               \
        SB();                                                                                          \
        wg_barrier();                                                          \
        LAS char* pe = opaque(lds + L_H + lpe); LAS char* po = opaque(lds + L_H + lpo);                \
        UNROLL for (int s = 0; s < 8; ++s)                                                             \
            UNROLL for (int tt = 0; tt < 4; ++tt) hf[s][tt] = FRAG(pe, po, tt, s);                     \
        SB();                                                                                          \
        f32x4 ac[4] = {};                                                                              \
        UNROLL for (int s = 0; s < 12; ++s) {                                                          \
            UNROLL for (int tt = 0; tt < 4; ++tt) {                                                    \
                ac[tt] = MFMA_EXPR;                                                                    \
                if (s + 8 < 12) hf[s + 8][tt] = FRAG(pe, po, tt, s + 8);                               \
                SB();                                                                                  \
            }                                                                                          \
        }
    if (w4 == 3) {
        wg_barrier();
        VMWAIT(0);
        STAMP(8);
        const u32x4 ones = {0x3f803f80u, 0x3f803f80u, 0x3f803f80u, 0x3f803f80u};
#pragma unroll 1
        for (int t = 0; t < 4; ++t) {
            const int tile = (wave >> 2) * 4 + t;
            LAS char* pe = lds + L_XK + tile * 12288 + lpe; LAS char* po = lds + L_XK + tile * 12288 + lpo;
            u32x4 f[12];
            UNROLL for (int s = 0; s < 12; ++s) f[s] = FRAG(pe, po, 0, s);
            SB();
            f32x4 sm = {}, gr = {};
            UNROLL for (int s = 0; s < 12; ++s) { sm = mfma16(ones, f[s], sm); gr = mfma16(f[s], f[s], gr); }
            const float d = (r & 2) ? ((r & 1) ? gr[3] : gr[2]) : ((r & 1) ? gr[1] : gr[0]);
            const f32x2 st = ln_coef(sm[0], d);
            if (g == (r >> 2)) { *(LAS float*)(lds + L_KST + (tile * 16 + r) * 4) = st[0]; *(LAS float*)(lds + L_KST + 512 + (tile * 16 + r) * 4) = st[1]; }
        }
        STAMP(9);
    } else if (wave > 3) {
        __builtin_amdgcn_s_setprio(2);
        const f32x4 qs = F4(lds, L_MOD + 1536 + (16 * ct + 4 * g) * 4) * (1.4426950408889634f * 0.05103103630798288f);
        STAGE1_GEMM(WS_MF + ct * 12288, mfma16(mf[s], hf[s][tt], ac[tt]))
        const unsigned vo = ((2 * (ct & 1) + (g >> 1)) * 16 + r) * 16 + (g & 1) * 8;
        UNROLL for (int tt = 0; tt < 4; ++tt) {
            const f32x4 v = ac[tt] * qs;
            u32x2 o = {pack2(v.xy), pack2(v.zw)};
            ST8(o, vo, WS_QF + ((4 * qbg + tt) * 12 + (ct >> 1)) * 1024);
        }
        __builtin_amdgcn_s_setprio(0);
        STAMP(9);
        VMWAIT(0);
        if (lane < 8) put_flag(ws, WS_IN1Q + (qbg * 8 + lane) * 128 + ct * 4, tag);
    } else {
        STAGE1_GEMM(WS_MF + 294912 + ct * 12288, mfma16(hf[s][tt], mf[s], ac[tt]))
        const int vw = j >> 1, vc = ct - vw * 6;
        const unsigned vo = (g >> 1) * 1024 + ((g & 1) * 32 + 16 * (ct & 1) + r) * 16;
        UNROLL for (int tp = 0; tp < 2; ++tp) {
            u32x4 o = {pack2(ac[2 * tp].xy), pack2(ac[2 * tp].zw), pack2(ac[2 * tp + 1].xy), pack2(ac[2 * tp + 1].zw)};
            ST16(o, vo, WS_VF + ((((qbg >> 1) * 4 + vw) * 3 + (vc >> 1)) * 8 + 4 * (qbg & 1) + 2 * tp) * 1024);
        }
        STAMP(9);
        VMWAIT(0);
        if (lane < 32) put_flag(ws, WS_IN1V + ((b * 16 + (lane >> 1)) * 8 + 2 * vw + (lane & 1)) * 512 + ((qbg >> 1) & 7) * 64 + ((qbg & 1) * 6 + vc) * 4, tag);
    }
    STAMP(10);
    wg_barrier();
    STAMP(11);

    if (wave > 3) {
        LAS char* pe0 = opaque(lds + L_XK + lpe); LAS char* po0 = opaque(lds + L_XK + lpo);
        LAS char* pe1 = opaque(lds + L_XK + 49152 + lpe); LAS char* po1 = opaque(lds + L_XK + 49152 + lpo);
        u32x4 kf[12][8], qf[12];
        f32x4 krs[8], kmu[8];
#define KF(s, t) ((t) < 4 ? FRAG(pe0, po0, (t), (s)) : FRAG(pe1, po1, (t) - 4, (s)))
        UNROLL for (int s = 0; s < 4; ++s)
            UNROLL for (int t = 0; t < 8; ++t) kf[s][t] = KF(s, t);
        LAS char* pst = opaque(lds + L_KST + g * 16);
        SB();
        poll_flags(ws, WS_IN1Q + cid * 128 + min(lane, 23) * 4, tag);
        __builtin_amdgcn_s_setprio(2);
        STAMP(12);
        UNROLL for (int s = 0; s < 12; ++s) qf[s] = LD16(lane * 16, WS_QF + ((4 * qbg + w4) * 12 + s) * 1024);
        SB();
        f32x4 sa[8] = {};
        float rq0 = 0.f, rq1 = 0.f;
        UNROLL for (int s = 0; s < 12; ++s) {
            UNROLL for (int t = 0; t < 8; ++t) {
                sa[t] = mfma16(kf[s][t], qf[s], sa[t]);
                if (s + 4 < 12) kf[s + 4][t] = KF(s + 4, t);
                else if (s == 9) krs[t] = F4(pst, t * 64);
                else if (s == 10) kmu[t] = F4(pst, 512 + t * 64);
                if (t == 0) rq0 = dot2(qf[s][0], 0x3f803f80u, rq0);
                if (t == 1) rq1 = dot2(qf[s][1], 0x3f803f80u, rq1);
                if (t == 2) rq0 = dot2(qf[s][2], 0x3f803f80u, rq0);
                if (t == 3) rq1 = dot2(qf[s][3], 0x3f803f80u, rq1);
                if (s == 11 && t == 4) rq0 = rows_sum(rq0 + rq1);
                SB();
            }
        }
        STAMP(13);
        float mx = -3.0e38f;
        UNROLL for (int t = 0; t < 8; ++t) {
            sa[t] = sa[t] * krs[t] + kmu[t] * rq0;
            mx = fmaxf(fmaxf(fmaxf(mx, sa[t][0]), sa[t][1]), fmaxf(sa[t][2], sa[t][3]));
        }
        mx = rows_max(mx);
        f32x4 ls = {};
        const unsigned vo = (g >> 1) * 1024 + ((g & 1) * 32 + (w4 & 1) * 16 + r) * 16;
        UNROLL for (int tp = 0; tp < 4; ++tp) {
            UNROLL for (int t = 2 * tp; t < 2 * tp + 2; ++t) {
                sa[t] -= mx;
                UNROLL for (int e = 0; e < 4; ++e) sa[t][e] = __builtin_amdgcn_exp2f(sa[t][e]);
                ls += sa[t];
            }
            u32x4 o = {pack2(sa[2 * tp].xy), pack2(sa[2 * tp].zw), pack2(sa[2 * tp + 1].xy), pack2(sa[2 * tp + 1].zw)};
            ST16(o, vo, WS_PF + ((cid * 2 + (w4 >> 1)) * 8 + 2 * tp) * 1024);
        }
        const float l = rows_sum((ls[0] + ls[1]) + (ls[2] + ls[3]));
        const u32x2 ml = {__float_as_uint(mx), __float_as_uint(l)};
        if (g == 0) ST8(ml, r * 8, WS_ML + cid * 512 + w4 * 128);
        __builtin_amdgcn_s_setprio(0);
        STAMP(14);
        wg_barrier();
    } else {
        poll_flags(ws, WS_IN1V + cid * 512 + (wave + 4 * (lane >> 5)) * 64 + min(lane & 31, 11) * 4, tag);
        STAMP(12);
    }

    u32x4 vf[24];
    UNROLL for (int i = 0; i < 24; ++i) vf[i] = LD16(lane * 16, WS_VF + (((b * 8 + wave) * 4 + (j >> 1)) * 24 + i) * 1024);
    SB();
    if (wave > 3) {
        VMWAIT(24);
        if (lane < 4) put_flag(ws, WS_IN2 + (qbg * 8 + 2 * lane + (w4 >> 1)) * 128 + (j * 2 + (w4 & 1)) * 4, tag);
        STAMP(15);
    } else wg_barrier();
    const int oq = (tid * 43691) >> 19, oc = tid - 12 * oq;
    const int ooff = (64 * qbg + 32 * (j & 1) + oq) * D + 96 * (j >> 1) + 8 * oc;
    u32x4 xr = {};
    if (wave < 6) xr = *(const u32x4*)(x + ooff);
    poll_flags(ws, WS_IN2 + cid * 128 + (wave * 2 + (lane & 1)) * 4, tag);
    STAMP(16);
    {
        u32x4 pf[8];
        const int prod = qbg * 8 + wave;
        UNROLL for (int ks = 0; ks < 8; ++ks) pf[ks] = LD16(lane * 16, WS_PF + ((prod * 2 + (j & 1)) * 8 + ks) * 1024);
        const u32x2 ml = __builtin_amdgcn_raw_buffer_load_b64(rs, (lane & 31) * 8, WS_ML + prod * 512 + (j & 1) * 256, 16);
        SB();
        LAS char* pw = lds + L_XK + (wave * 32 + (lane & 31)) * PROW + (lane >> 5) * 16;
        f32x16 oa[3];
        UNROLL for (int dt = 0; dt < 4; ++dt) {
            if (dt < 3) {
                oa[dt] = (f32x16){};
                UNROLL for (int ks = 0; ks < 8; ++ks) oa[dt] = mfma32(vf[dt * 8 + ks], pf[ks], oa[dt]);
            }
            if (dt > 0) {
                const f32x16& a = oa[dt - 1];
                UNROLL for (int i = 0; i < 2; ++i) {
                    auto r0 = __builtin_amdgcn_permlane32_swap(pack2(a[8 * i], a[8 * i + 1]), pack2(a[8 * i + 4], a[8 * i + 5]), false, false);
                    auto r1 = __builtin_amdgcn_permlane32_swap(pack2(a[8 * i + 2], a[8 * i + 3]), pack2(a[8 * i + 6], a[8 * i + 7]), false, false);
                    U4(pw, (dt - 1) * 64 + i * 32) = (u32x4){r0[0], r1[0], r0[1], r1[1]};
                }
            }
            if (dt == 1) { if (lane < 32) *(LAS u32x2*)(lds + L_ML + (wave * 32 + lane) * 8) = ml; }
            if (dt > 0 && dt < 3) {
                __builtin_amdgcn_sched_group_barrier(0x008, 2, 0);
                UNROLL for (int k = 0; k < 6; ++k) { __builtin_amdgcn_sched_group_barrier(0x282, 3, 0); __builtin_amdgcn_sched_group_barrier(0x008, 1, 0); }
            }
            SB();
        }
    }
    STAMP(17);
    wg_barrier();
    STAMP(18);
    if (wave < 6) {
        f32x2 ml[8]; u32x4 pv[8];
        UNROLL for (int k = 0; k < 8; ++k) ml[k] = *(LAS f32x2*)(lds + L_ML + (k * 32 + oq) * 8);
        UNROLL for (int k = 0; k < 8; ++k) pv[k] = U4(lds, L_XK + (k * 32 + oq) * PROW + oc * 16);
        const f32x4 gt0 = F4(lds, L_MOD + 3072 + (96 * (j >> 1) + 8 * oc) * 4), gt1 = F4(lds, L_MOD + 3072 + (96 * (j >> 1) + 8 * oc) * 4 + 16);
        SB();
        float m = ml[0][0];
        UNROLL for (int k = 1; k < 8; ++k) m = fmaxf(m, ml[k][0]);
        float wk[8], L = 0.f;
        UNROLL for (int k = 0; k < 8; ++k) { wk[k] = __builtin_amdgcn_exp2f(ml[k][0] - m); L = fmaf(wk[k], ml[k][1], L); }
        const float inv = __builtin_amdgcn_rcpf(L);
        f32x2 o[4] = {};
        UNROLL for (int k = 0; k < 8; ++k)
            UNROLL for (int e = 0; e < 4; ++e) o[e] += unpack2(pv[k][e]) * wk[k];
        u32x4 w = {pack2(o[0] * inv * gt0.xy + unpack2(xr[0])), pack2(o[1] * inv * gt0.zw + unpack2(xr[1])),
                   pack2(o[2] * inv * gt1.xy + unpack2(xr[2])), pack2(o[3] * inv * gt1.zw + unpack2(xr[3]))};
        { const __amdgpu_buffer_rsrc_t ro = __builtin_amdgcn_make_buffer_rsrc(out, 0, 2048 * 768, 0x00020000); __builtin_amdgcn_raw_buffer_store_b128(w, ro, ooff * 2, 0, 16); }
        if ((cid & 127) == 0 && tid == 0) put_flag(ws, WS_EPOCH + b * 4, tag);
    }
    STAMP(29); STAMPC(31);
    STAMP_OUT;
    asm volatile(".Lpf_end:");
}

extern "C" void kernel_launch(void* const* d_in, const int* in_sizes, int n_in, void* d_out, int out_size, void* d_ws, size_t ws_size, hipStream_t stream) {
    dit_fused<<<256, 512, 0, stream>>>((const u16*)d_in[0], (const u16*)d_in[1], (const u16*)d_in[2], (const u16*)d_in[3], (const u16*)d_in[4], (u16*)d_out, (char*)d_ws);
}
```

```cpp
#include <hip/hip_runtime.h>
#include <hip/hip_bf16.h>

typedef unsigned short u16;
typedef unsigned long long u64;
typedef __bf16 bf16x2 __attribute__((ext_vector_type(2)));
typedef __bf16 bf16x8 __attribute__((ext_vector_type(8)));
typedef short s16x4 __attribute__((ext_vector_type(4)));
typedef float f32x2 __attribute__((ext_vector_type(2)));
typedef float f32x4 __attribute__((ext_vector_type(4)));
typedef float f32x16 __attribute__((ext_vector_type(16)));
typedef unsigned u32x2 __attribute__((ext_vector_type(2)));
typedef unsigned u32x4 __attribute__((ext_vector_type(4)));
#define GAS __attribute__((address_space(1)))
#define LAS __attribute__((address_space(3)))
#define DEV __device__ __forceinline__
#define RLX_AGENT __ATOMIC_RELAXED, __HIP_MEMORY_SCOPE_AGENT
#define SB() __builtin_amdgcn_sched_barrier(0)
#define UNROLL _Pragma("unroll")

constexpr int D = 384;
constexpr unsigned WS_EPOCH = 0;
constexpr unsigned WS_MODG = 256;
constexpr unsigned WS_F0 = WS_MODG + 8 * 9216;
constexpr unsigned WS_CNT = WS_F0 + 2 * 288 * 1024;
constexpr unsigned WS_CQ = WS_CNT, WS_CV = WS_CQ + 32 * 128, WS_CP = WS_CV + 256 * 128;
constexpr unsigned WS_TST = WS_CNT + 544 * 128;
constexpr unsigned WS_MF = WS_TST + 4 * 32768;
constexpr unsigned WS_QF = WS_MF + 2 * 294912;
constexpr unsigned WS_VF = WS_QF + 1572864;
constexpr unsigned WS_PF = WS_VF + 1572864;
constexpr unsigned WS_ML = WS_PF + 256 * 16384;
constexpr unsigned WS_END = WS_ML + 256 * 512;
constexpr unsigned WS_STAMP = WS_END;
constexpr int L_XK = 0;
constexpr int L_SA = 73728, L_SB = 98304;
constexpr int L_H = 98304;
constexpr int L_MOD = 147456;
constexpr int L_KST = 152064;
constexpr int L_WT = 153088;
constexpr int L_CNT = 155136;
constexpr int L_TOTAL = 155168;
constexpr int SPIN_CAP = 1 << 16;
constexpr int NWAVES = 8;
#define R3DELAY 40
constexpr int PPITCH = 112;
#define PRE 10
#define ABL 0
#define PWARM 1
#define PGAP 5
#define PSLEEP 2
#define THR 16
#define VDELAY 14

#define STAMP(i) do {} while (0)
#define STAMPC(i) do {} while (0)
#define STAMP_DECL do {} while (0)
#define STAMP_OUT do {} while (0)

DEV void wg_barrier() { asm volatile("s_waitcnt lgkmcnt(0)\n\ts_barrier" ::: "memory"); }
#define VMWAIT(n) asm volatile("s_waitcnt vmcnt(" #n ")" ::: "memory")
DEV unsigned pack2(float a, float b) { f32x2 f = {a, b}; return __builtin_bit_cast(unsigned, __builtin_convertvector(f, bf16x2)); }
DEV unsigned pack2(f32x2 f) { return __builtin_bit_cast(unsigned, __builtin_convertvector(f, bf16x2)); }
DEV float blo(unsigned u) { return __uint_as_float(u << 16); }
DEV float bhi(unsigned u) { return __uint_as_float(u & 0xffff0000u); }
DEV f32x2 unpack2(unsigned u) { return (f32x2){blo(u), bhi(u)}; }
DEV float dot2(unsigned a, unsigned b, float c) { return __builtin_amdgcn_fdot2_f32_bf16(__builtin_bit_cast(bf16x2, a), __builtin_bit_cast(bf16x2, b), c, false); }
DEV f32x4 mfma16(u32x4 a, u32x4 b, f32x4 c) { return __builtin_amdgcn_mfma_f32_16x16x32_bf16(__builtin_bit_cast(bf16x8, a), __builtin_bit_cast(bf16x8, b), c, 0, 0, 0); }
DEV f32x16 mfma32(u32x4 a, u32x4 b, f32x16 c) { return __builtin_amdgcn_mfma_f32_32x32x16_bf16(__builtin_bit_cast(bf16x8, a), __builtin_bit_cast(bf16x8, b), c, 0, 0, 0); }
DEV float rows_sum(float v) {
    auto a = __builtin_amdgcn_permlane16_swap(__float_as_uint(v), __float_as_uint(v), false, false);
    v = __uint_as_float(a[0]) + __uint_as_float(a[1]);
    auto b = __builtin_amdgcn_permlane32_swap(__float_as_uint(v), __float_as_uint(v), false, false);
    return __uint_as_float(b[0]) + __uint_as_float(b[1]);
}
DEV float rows_max(float v) {
    auto a = __builtin_amdgcn_permlane16_swap(__float_as_uint(v), __float_as_uint(v), false, false);
    v = fmaxf(__uint_as_float(a[0]), __uint_as_float(a[1]));
    auto b = __builtin_amdgcn_permlane32_swap(__float_as_uint(v), __float_as_uint(v), false, false);
    return fmaxf(__uint_as_float(b[0]), __uint_as_float(b[1]));
}
DEV float oct_sum(float v) {
    v += __int_as_float(__builtin_amdgcn_mov_dpp(__float_as_int(v), 0xB1, 0xf, 0xf, true));
    v += __int_as_float(__builtin_amdgcn_mov_dpp(__float_as_int(v), 0x4E, 0xf, 0xf, true));
    v += __int_as_float(__builtin_amdgcn_mov_dpp(__float_as_int(v), 0x141, 0xf, 0xf, true));
    return v;
}
DEV f32x2 ln_coef(float sum, float sq) {
    const float mu = sum * (1.f / D);
    const float rstd = __builtin_amdgcn_rsqf(fmaxf(sq * (1.f / D) - mu * mu, 0.f) + 1e-5f);
    return (f32x2){rstd, -mu * rstd};
}
DEV void poll_flags(char* ws, unsigned off, unsigned tag, bool active) {
    GAS unsigned* p = (GAS unsigned*)(ws + off);
    bool pend = active;
#pragma unroll 1
    for (int spin = 0; spin < SPIN_CAP; ++spin) {
        if (pend) pend = __hip_atomic_load(p, RLX_AGENT) != tag;
        if (!__any(pend)) break;
        __builtin_amdgcn_s_sleep(PSLEEP);
    }
    asm volatile("" ::: "memory");
}
DEV void lds_arrive(LAS char* p, int lane) {
    asm volatile("s_waitcnt lgkmcnt(0)" ::: "memory");
    if (lane == 0) __hip_atomic_fetch_add((LAS unsigned*)p, 1u, __ATOMIC_RELAXED, __HIP_MEMORY_SCOPE_WORKGROUP);
}
DEV void lds_wait(LAS char* p, unsigned n) {
#pragma unroll 1
    for (int spin = 0; spin < SPIN_CAP * 16; ++spin) {
        if (__hip_atomic_load((LAS unsigned*)p, __ATOMIC_RELAXED, __HIP_MEMORY_SCOPE_WORKGROUP) >= n) break;
        __builtin_amdgcn_s_sleep(1);
    }
    asm volatile("" ::: "memory");
}
DEV void lds_spin(LAS char* p) {
#pragma unroll 1
    for (int spin = 0; spin < SPIN_CAP * 64; ++spin)
        if (__hip_atomic_load((LAS unsigned*)p, __ATOMIC_RELAXED, __HIP_MEMORY_SCOPE_WORKGROUP)) break;
    asm volatile("" ::: "memory");
}
#define PISSUE(x) asm volatile("global_atomic_or %0, %1, %2, %3 sc0 sc1" : "+v"(x) : "v"(off), "v"(0u), "s"(ws) : "memory")
#define PSTEP(x) asm volatile("s_waitcnt vmcnt(2)" : "+v"(x) :: "memory"); if (__all(x >= n)) break; PISSUE(x)
DEV void poll_count(char* ws, unsigned off, unsigned n, bool active, unsigned& pa, unsigned& pb, unsigned& pc) {
    if (active) {
        PISSUE(pa); __builtin_amdgcn_s_sleep(PGAP); PISSUE(pb); __builtin_amdgcn_s_sleep(PGAP); PISSUE(pc);
#pragma unroll 1
        for (int spin = 0; spin < SPIN_CAP; ++spin) { PSTEP(pa); PSTEP(pb); PSTEP(pc); }
    }
    asm volatile("" ::: "memory");
}
DEV void count_up(char* ws, unsigned off) { asm volatile("" ::: "memory"); __hip_atomic_fetch_add((GAS unsigned*)(ws + off), 1u, RLX_AGENT); }
DEV unsigned max4(unsigned m, u32x4 v) { return max(max(max(m, v[0]), v[1]), max(v[2], v[3])); }
constexpr unsigned EMPTY = 0xffffffffu;
DEV void put_flag(char* ws, unsigned off, unsigned tag) { __hip_atomic_store((GAS unsigned*)(ws + off), tag, RLX_AGENT); }
DEV void glds(unsigned voff, const char* sbase, unsigned ldsdst) {
    asm volatile("s_mov_b32 m0, %2\n\ts_nop 0\n\tglobal_load_lds_dwordx4 %0, %1" :: "v"(voff), "s"(sbase), "s"(ldsdst) : "memory");
}
#define U4(p, off) (*(LAS u32x4*)((p) + (off)))
#define F4(p, off) (*(LAS f32x4*)((p) + (off)))
#define LD16(voff, soff) __builtin_amdgcn_raw_buffer_load_b128(rs, (voff), (soff), 16)
#define LDF(base, i) LD16(lane * 16 + ((i) & 3) * 1024, (base) + ((i) >> 2) * 4096)
#define ST16(v, voff, soff) __builtin_amdgcn_raw_buffer_store_b128((v), rs, (voff) + (soff), 0, 16)
#define ST8(v, voff, soff) __builtin_amdgcn_raw_buffer_store_b64((v), rs, (voff) + (soff), 0, 16)
#define WARM(voff) asm volatile("global_load_dword %0, %1, %2 sc1" : "+v"(warm_sink) : "v"(voff), "s"(ws) : "memory")
DEV u32x4 tr_frag(LAS char* p0, LAS char* p1) {
    const u32x2 l2 = __builtin_bit_cast(u32x2, __builtin_amdgcn_ds_read_tr16_b64_v4i16((LAS s16x4*)p0));
    const u32x2 h2 = __builtin_bit_cast(u32x2, __builtin_amdgcn_ds_read_tr16_b64_v4i16((LAS s16x4*)p1));
    return (u32x4){l2[0], l2[1], h2[0], h2[1]};
}
DEV LAS char* opaque(LAS char* p) { asm volatile("" : "+v"(p)); return p; }
#define FRAG(pe, po, t, s) U4(((s) & 1) ? (po) : (pe), ((t) * 12 + ((s) >> 1)) * 1024)

extern "C" __global__ __launch_bounds__(512, 2) void dit_fused(const u16* __restrict__ x, const u16* __restrict__ cvec, const u16* __restrict__ wqkv,
                                                               const u16* __restrict__ wo, const u16* __restrict__ wmod, u16* __restrict__ out, char* ws) {
    __shared__ __attribute__((aligned(16))) char smem[L_TOTAL];
    LAS char* lds = (LAS char*)smem;
    const unsigned lds0 = (unsigned)(size_t)lds;
    const int tid = threadIdx.x ^ 256, lane = tid & 63;
    const int wave = __builtin_amdgcn_readfirstlane(tid >> 6);
    const int bid = blockIdx.x, xg = bid & 7, idx = bid >> 3;
    STAMP_DECL;
    STAMP(0); STAMPC(30);
    unsigned pf0;
    {
        u64 pc; unsigned len;
        asm volatile("s_getpc_b64 %0\n.Lpf_here:\n\ts_mov_b32 %1, .Lpf_end-.Lpf_here" : "=s"(pc), "=s"(len));
        unsigned o0 = min((unsigned)(((idx & 3) * 8 + wave) * 8 + (lane >> 3)) * 128u, len);
        asm volatile("global_load_dword %0, %1, %2" : "=&v"(pf0) : "v"(o0), "s"(pc) : "memory");
    }
    if (tid < 8) *(LAS unsigned*)(lds + L_CNT + tid * 4) = 0u;
    asm volatile("s_waitcnt lgkmcnt(0)" :: "s"(x), "s"(cvec), "s"(wqkv), "s"(wo), "s"(wmod), "s"(out), "s"(ws) : "memory");
    u64 ep;
    asm volatile("s_load_dwordx2 %0, %1, 0x0" : "=s"(ep) : "s"(ws) : "memory");

    const int b = xg >> 2;
    const int qbg = b * 16 + (xg & 3) * 4 + (idx >> 3);
    const int j = idx & 7;
    const int cid = qbg * 8 + j;
    const int r = lane & 15, g = lane >> 4, row8 = lane >> 3, sl = lane & 7;
    const unsigned swz = (unsigned)(sl ^ row8) * 16u;
    const int slice = xg + 8 * (idx - 18);
    const int role = idx < 9 ? 0 : idx < 18 ? 1 : slice < 18 ? 2 : 3;
    const int w4 = wave & 3;
    const int ti = idx - (role == 1 ? 9 : 0);
    const int t3 = (ti * 11) >> 5;
    const int s0 = 3 * (xg >> 1) + t3, cb = 3 * (xg & 1) + (ti - 3 * t3);

    const __amdgpu_buffer_rsrc_t rs = __builtin_amdgcn_make_buffer_rsrc(ws, 0, WS_END, 0x00020000);
    const u32x4 empty4 = {EMPTY, EMPTY, EMPTY, EMPTY};
    unsigned pa = 0, pb = 0, pc_ = 0;
    u32x2 craw = {};
    if (role < 3) {
        if (role == 2) {
            const int c = (slice * 4 + w4) * 12 + lane;
            if (wave > 3 && lane < 12 && c < 544) put_flag(ws, WS_CNT + c * 128, 0u);
            asm volatile("global_load_dwordx2 %0, %1, %2" : "=v"(craw) : "v"(min(tid & 255, 191) * 8), "s"(cvec) : "memory");
        } else {
            const int rg = wave >> 1, c0 = 3 * (wave & 1);
            const char* ap = (const char*)wqkv + (32 * s0 + 16 * (rg & 1) + 4 * (rg >> 1)) * 2304 + role * 1536 + c0 * 128;
            const unsigned va = (8 * (row8 >> 2) + (row8 & 3)) * 2304 + swz;
            UNROLL for (int i = 0; i < 3; ++i) glds(va, ap + i * 128, lds0 + L_SA + (rg * 6 + c0 + i) * 1024);
        }
        const int ldb = role == 0 ? 2304 : role == 1 ? 768 : 4608;
        const char* bp = role == 0 ? (const char*)wqkv + 768 + (64 * cb + 8 * wave) * 2304
                       : (role == 1 ? (const char*)wo + cb * 128 : (const char*)wmod + slice * 128) + 48 * wave * ldb;
        const int stepb = role == 0 ? 128 : 8 * ldb;
        const unsigned vb = row8 * ldb + swz;
        const unsigned vb1 = vb ^ (role ? 64u : 0u);
        UNROLL for (int i = 0; i < 6; ++i) glds((i & 1) ? vb1 : vb, bp + i * stepb, lds0 + L_SB + (wave * 6 + i) * 1024);
    }
    const int lpe = (r >> 3) * 6144 + (r & 7) * 128 + ((g ^ (r & 7)) * 16), lpo = lpe ^ 64;
    const unsigned vk = row8 * 768 + swz;
    const char* kp = (const char*)x + (b * 1024 + 128 * j) * 768;
#define KEY_DMA(rg0, n) do { if (!(ABL & 2)) _Pragma("unroll 1") for (int rg = (rg0); rg < (rg0) + (n); ++rg) { \
        UNROLL for (int i = 0; i < 6; ++i) glds(vk, kp + rg * 6144 + i * 128, lds0 + L_XK + (rg * 6 + i) * 1024); } } while (0)
    unsigned tag0, tag1, tag;
#define GET_TAGS() do { u64 e_; asm volatile("s_waitcnt lgkmcnt(0)\n\ts_mov_b64 %0, %1" : "=s"(e_) : "s"(ep)); \
        tag0 = (unsigned)e_ + 1u; tag1 = (unsigned)(e_ >> 32) + 1u; tag = b ? tag1 : tag0; } while (0)
    const int ct = 3 * j + w4;
#define STAGE1_GEMM(MFMA_EXPR)                                                                         \
        u32x4 hf[12][4];                                                                               \
        LAS char* pe = opaque(lds + L_H + lpe); LAS char* po = opaque(lds + L_H + lpo);                \
        UNROLL for (int s = 0; s < 3; ++s)                                                             \
            UNROLL for (int tt = 0; tt < 4; ++tt) hf[s][tt] = FRAG(pe, po, tt, s);                     \
        SB();                                                                                          \
        f32x4 ac[4] = {};                                                                              \
        UNROLL for (int s = 0; s < 12; ++s) {                                                          \
            UNROLL for (int tt = 0; tt < 4; ++tt) {                                                    \
                ac[tt] = MFMA_EXPR;                                                                    \
                if (s + 3 < 12) hf[s + 3][tt] = FRAG(pe, po, tt, s + 3);                               \
                SB();                                                                                  \
            }                                                                                          \
        }
    unsigned warm_sink = 0;
    const int oq = (tid * 43691) >> 18, oc = tid - 6 * oq;
    const int ooff = (64 * qbg + oq) * D + 48 * j + 8 * oc;
    u32x4 xr = {};
    asm volatile("s_barrier" ::: "memory");

    if (wave > 3) {
        const unsigned mo = WS_MF + role * 294912 + ((4 * cb + w4) * 12 + s0) * 1024 + lane * 16;
        if (role < 2) ST16(empty4, mo, 0);
        STAMP(1);
        GET_TAGS();
        if (role == 2) {
            asm volatile("s_waitcnt vmcnt(6)" : "+v"(craw) :: "memory");
            u32x2 o;
            UNROLL for (int e = 0; e < 2; ++e) {
                float a = blo(craw[e]), c = bhi(craw[e]);
                a = a * __builtin_amdgcn_rcpf(1.f + __builtin_amdgcn_exp2f(-1.4426950408889634f * a));
                c = c * __builtin_amdgcn_rcpf(1.f + __builtin_amdgcn_exp2f(-1.4426950408889634f * c));
                o[e] = pack2(a, c);
            }
            const int t = tid - 256, row = t >= 96, q4 = t - 96 * row, ch = q4 >> 1;
            if (t < 192) *(LAS u32x2*)(lds + L_SA + (ch >> 3) * 1024 + row * 128 + (((ch & 7) ^ row) * 16) + (q4 & 1) * 8) = o;
        }
        if (role < 3) {
            VMWAIT(0);
            wg_barrier();
            STAMP(2);
            u32x4 a0[12], a1[12], bf[12];
            LAS char* pe = opaque(lds + L_SA + lpe); LAS char* po = opaque(lds + L_SA + lpo);
            f32x4 lo = {}, hi = {};
#define S0_LOOP(BLOAD)                                                                                 \
            UNROLL for (int s = 0; s < 16; ++s) {                                                      \
                if (s < 12) { BLOAD; a0[s] = FRAG(pe, po, 0, s); a1[s] = FRAG(pe, po, 1, s); }         \
                if (s >= 4) { lo = mfma16(a0[s - 4], bf[s - 4], lo); hi = mfma16(a1[s - 4], bf[s - 4], hi); } \
                SB();                                                                                  \
            }
            if (role == 0) {
                LAS char* qe = opaque(lds + L_SB + w4 * 12288 + lpe); LAS char* qo = opaque(lds + L_SB + w4 * 12288 + lpo);
                S0_LOOP(bf[s] = FRAG(qe, qo, 0, s))
            } else {
                const int q = (lane >> 2) & 3, p = lane & 3;
                LAS char* t0 = opaque(lds + L_SB + g * 1024 + q * 128 + (((2 * w4 + (p >> 1)) ^ q ^ (4 * (g & 1))) * 16) + 8 * (p & 1));
                LAS char* t1 = opaque(lds + L_SB + g * 1024 + (4 + q) * 128 + (((2 * w4 + (p >> 1)) ^ (4 + q) ^ (4 * (g & 1))) * 16) + 8 * (p & 1));
                S0_LOOP(bf[s] = tr_frag(t0 + s * 4096, t1 + s * 4096))
            }
            if (role < 2) {
                u32x4 o = {pack2(lo.xy), pack2(lo.zw), pack2(hi.xy), pack2(hi.zw)};
                ST16(o, mo, 0);
                asm volatile("" ::: "memory");
                if (lane < 8) put_flag(ws, WS_F0 + (lane * 576 + (role * 24 + 4 * cb + w4) * 12 + s0) * 128, lane < 4 ? tag0 : tag1);
            } else {
                const unsigned u = pack2(lo.xy);
                const unsigned u0 = __shfl(u, 2 * sl), u1 = __shfl(u, 2 * sl + 1);
                const unsigned pay = lane < 32 ? ((u0 & 0xffffu) | (u1 << 16)) : ((u0 >> 16) | (u1 & 0xffff0000u));
                __hip_atomic_store((GAS u64*)(ws + WS_MODG + row8 * 9216 + (4 * slice + w4) * 128 + sl * 8), ((u64)(lane < 32 ? tag0 : tag1) << 32) | pay, RLX_AGENT);
            }
            STAMP(3);
            lds_arrive(lds + L_CNT + 24, lane);
        }
        unsigned qo = WS_QF + (48 * qbg + (ct >> 1)) * 1024 + (g & 1) * 12288 + ((2 * (ct & 1) + (g >> 1)) * 16 + r) * 16;
        unsigned po_ = WS_PF + (cid * 4 + w4) * 4096 + lane * 16;
        const unsigned mlo = WS_ML + cid * 512 + w4 * 128 + r * 8;
        asm volatile("" : "+v"(qo), "+v"(po_));
        if (role == 3) __builtin_amdgcn_s_sleep(R3DELAY);
        KEY_DMA(2 * w4, 2);
        if (role == 3) KEY_DMA(8 + 2 * w4, 2);
        STAMP(4);
        if (wave < 7) {
            poll_flags(ws, WS_F0 + (xg * 576 + g * 288 + ct * 12 + r) * 128, tag, g < 2 && r < 12);
            asm volatile("" :: "v"(pf0));
            STAMP(6);
            u32x4 mf[12];
#pragma unroll 1
            for (int spin = 0; spin < SPIN_CAP; ++spin) {
                UNROLL for (int s = 0; s < 12; ++s) mf[s] = LDF(WS_MF + ct * 12288, s);
                SB();
                unsigned m = 0;
                UNROLL for (int s = 0; s < 12; ++s) m = max4(m, mf[s]);
                if (!__any(m == EMPTY)) break;
                asm volatile("" ::: "memory");
            }
            ST16(empty4, qo, 0); ST16(empty4, qo, 24576);
            STAMP(7);
            wg_barrier();
            STAMP(8);
            const f32x4 qs = F4(lds, L_MOD + 1536 + (16 * ct + 4 * g) * 4) * (1.4426950408889634f * 0.05103103630798288f);
            STAGE1_GEMM(mfma16(mf[s], hf[s][tt], ac[tt]))
            VMWAIT(0);
            UNROLL for (int tt = 0; tt < 4; tt += 2) {
                const f32x4 v0 = ac[tt] * qs, v1 = ac[tt + 1] * qs;
                auto r0 = __builtin_amdgcn_permlane16_swap(pack2(v0.xy), pack2(v1.xy), false, false);
                auto r1 = __builtin_amdgcn_permlane16_swap(pack2(v0.zw), pack2(v1.zw), false, false);
                u32x4 o = {r0[0], r1[0], r0[1], r1[1]};
                ST16(o, qo, tt * 12288);
            }
            if (lane == 0) count_up(ws, WS_CQ + qbg * 128);
            lds_arrive(lds + L_CNT + 12, lane);
            STAMP(9);
        } else {
            if (role < 3) { lds_wait(lds + L_CNT + 24, 4); KEY_DMA(8, 8); }
            VMWAIT(0);
            asm volatile("" :: "v"(pf0));
            STAMP(7);
            wg_barrier();
            GAS u64* gp = (GAS u64*)(ws + WS_TST + (xg & 3) * 32768 + (b * 1024 + 128 * j) * 16) + lane;
            u64 g0, g1, g2, g3;
#pragma unroll 1
            for (int spin = 0; spin < SPIN_CAP; ++spin) {
                g0 = __hip_atomic_load(gp, RLX_AGENT); g1 = __hip_atomic_load(gp + 64, RLX_AGENT); g2 = __hip_atomic_load(gp + 128, RLX_AGENT); g3 = __hip_atomic_load(gp + 192, RLX_AGENT);
                if (__all((unsigned)(g0 >> 32) == tag && (unsigned)(g1 >> 32) == tag && (unsigned)(g2 >> 32) == tag && (unsigned)(g3 >> 32) == tag)) break;
                __builtin_amdgcn_s_sleep(2);
            }
            LAS char* pk = lds + L_KST + (lane & 1) * 512 + (lane >> 1) * 4;
            *(LAS unsigned*)(pk) = (unsigned)g0; *(LAS unsigned*)(pk + 128) = (unsigned)g1; *(LAS unsigned*)(pk + 256) = (unsigned)g2; *(LAS unsigned*)(pk + 384) = (unsigned)g3;
        }
        UNROLL for (int tp = 0; tp < 4; ++tp) ST16(empty4, po_, tp * 1024);
        if (g == 0) ST8(empty4.xy, mlo, 0);
        SB();
        LAS char* pe0 = opaque(lds + L_XK + lpe); LAS char* po0 = opaque(lds + L_XK + lpo);
        LAS char* pe1 = opaque(lds + L_XK + 49152 + lpe); LAS char* po1 = opaque(lds + L_XK + 49152 + lpo);
        LAS char* pst = opaque(lds + L_KST + g * 16);
        f32x4 krs[8], kmu[8], sa[8];
        const u32x4 ones = {0x3f803f80u, 0x3f803f80u, 0x3f803f80u, 0x3f803f80u};
        const unsigned qbase = WS_QF + (4 * qbg + w4) * 12288;
#define KF(s, t) ((t) < 4 ? FRAG(pe0, po0, (t), (s)) : FRAG(pe1, po1, (t) - 4, (s)))
#pragma unroll 1
        for (int att = 0; att < SPIN_CAP; ++att) {
            u32x4 kf[12][8], qf[12];
            UNROLL for (int s = 0; s < 3; ++s)
                UNROLL for (int t = 0; t < 8; ++t) kf[s][t] = KF(s, t);
            SB();
            if (att == 0) {
                if (wave == 7) {
                    lds_wait(lds + L_CNT + 12, 3);
                    STAMP(10);
                    poll_count(ws, WS_CQ + qbg * 128, 24, lane == 0, pa, pb, pc_);
                    lds_arrive(lds + L_CNT + 16, lane);
                } else {
                    VMWAIT(0);
                    WARM(qo); WARM(qo + 24576);
                    STAMP(10);
                    lds_spin(lds + L_CNT + 16);
                }
                STAMP(12);
            }
            UNROLL for (int s = 0; s < 6; ++s) qf[s] = LDF(qbase, s);
            SB();
            STAMP(13);
            f32x4 rq = {};
            unsigned m = 0;
            UNROLL for (int t = 0; t < 8; ++t) sa[t] = (f32x4){};
            UNROLL for (int s = 0; s < 12; ++s) {
                rq = mfma16(ones, qf[s], rq);
                SB();
                UNROLL for (int t = 0; t < 8; ++t) {
                    sa[t] = mfma16(kf[s][t], qf[s], sa[t]);
                    if (s + 3 < 12) kf[s + 3][t] = KF(s + 3, t);
                    else if (s == 9) kmu[t] = F4(pst, 512 + t * 64);
                    else if (s == 10) krs[t] = F4(pst, t * 64);
                    else if (s == 11 && t > 0) { kmu[t - 1][0] *= rq[0]; kmu[t - 1][1] *= rq[0]; }
                    if (t == 0) m = max(max(m, qf[s][0]), qf[s][1]);
                    if (t == 1) m = max(max(m, qf[s][2]), qf[s][3]);
                    if (s < 3 && (t == 3 || t == 6)) qf[6 + 2 * s + (t == 6)] = LDF(qbase, 6 + 2 * s + (t == 6));
                    if (s == 3 && t == 3 && lane == 0) __hip_atomic_fetch_add((LAS unsigned*)(lds + L_CNT + 4), 1u, __ATOMIC_RELAXED, __HIP_MEMORY_SCOPE_WORKGROUP);
                    SB();
                }
            }
            kmu[7][0] *= rq[0]; kmu[7][1] *= rq[0];
            UNROLL for (int t = 0; t < 8; ++t) { kmu[t][2] *= rq[0]; kmu[t][3] *= rq[0]; }
            if (!__any(m == EMPTY)) break;
            asm volatile("" ::: "memory");
        }
        STAMP(14);
        float mx = -3.0e38f;
        UNROLL for (int t = 0; t < 8; ++t) {
            sa[t] = sa[t] * krs[t] + kmu[t];
            mx = fmaxf(fmaxf(fmaxf(mx, sa[t][0]), sa[t][1]), fmaxf(sa[t][2], sa[t][3]));
        }
        mx = rows_max(mx);
        f32x4 ls = {};
        UNROLL for (int tp = 0; tp < 4; ++tp) {
            UNROLL for (int t = 2 * tp; t < 2 * tp + 2; ++t) {
                sa[t] -= mx;
                UNROLL for (int e = 0; e < 4; ++e) sa[t][e] = __builtin_amdgcn_exp2f(sa[t][e]);
                ls += sa[t];
            }
            u32x4 o = {pack2(sa[2 * tp].xy), pack2(sa[2 * tp].zw), pack2(sa[2 * tp + 1].xy), pack2(sa[2 * tp + 1].zw)};
            ST16(o, po_, tp * 1024);
        }
        const float l = rows_sum((ls[0] + ls[1]) + (ls[2] + ls[3]));
        const u32x2 ml = {__float_as_uint(mx), __float_as_uint(l)};
        if (g == 0) ST8(ml, mlo, 0);
        if (lane == 0) count_up(ws, WS_CP + cid * 128);
        STAMP(15);
        VMWAIT(0);
        { const unsigned wo_ = WS_PF + (cid * 4 + w4) * 4096 + lane * 64; WARM(wo_); }
        if (wave < 6) xr = *(const u32x4*)(x + ooff);
        if ((cid & 127) == 0 && wave == 7) {
            GAS unsigned* p = (GAS unsigned*)(ws + WS_CQ + (b * 16 + r) * 128);
#pragma unroll 1
            for (int spin = 0; spin < SPIN_CAP; ++spin) { if (__all(__hip_atomic_load(p, RLX_AGENT) >= 24u)) break; __builtin_amdgcn_s_sleep(8); }
        }
        STAMP(16);
        lds_wait(lds + L_CNT + 8, 4);
        STAMP(17);
        u32x2 m0, m1;
#pragma unroll 1
        for (int spin = 0; spin < SPIN_CAP; ++spin) {
            m0 = __builtin_amdgcn_raw_buffer_load_b64(rs, g * 512 + r * 8, WS_ML + qbg * 4096 + w4 * 128, 16);
            m1 = __builtin_amdgcn_raw_buffer_load_b64(rs, g * 512 + r * 8, WS_ML + qbg * 4096 + 2048 + w4 * 128, 16);
            if (!__any(max(max(m0[0], m0[1]), max(m1[0], m1[1])) == EMPTY)) break;
            asm volatile("" ::: "memory");
        }
        const float gm = rows_max(fmaxf(__uint_as_float(m0[0]), __uint_as_float(m1[0])));
        const float w0 = __builtin_amdgcn_exp2f(__uint_as_float(m0[0]) - gm), w1 = __builtin_amdgcn_exp2f(__uint_as_float(m1[0]) - gm);
        const float inv = __builtin_amdgcn_rcpf(rows_sum(w0 * __uint_as_float(m0[1]) + w1 * __uint_as_float(m1[1])));
        *(LAS float*)(lds + L_WT + g * 256 + (16 * w4 + r) * 4) = w0 * inv;
        *(LAS float*)(lds + L_WT + 1024 + g * 256 + (16 * w4 + r) * 4) = w1 * inv;
        lds_arrive(lds + L_CNT + 20, lane);
        asm volatile("" :: "v"(warm_sink));
        STAMP(18);
    } else {
        u32x4 xq[12];
        {
            const char* xp = (const char*)x + (64 * qbg + 16 * wave) * 768;
            const unsigned vx = row8 * 768 + sl * 16, vx2 = vx + 6144;
#define XQ6(o, v) asm volatile("global_load_dwordx4 %0, %6, %7\n\tglobal_load_dwordx4 %1, %6, %7 offset:128\n\tglobal_load_dwordx4 %2, %6, %7 offset:256\n\t" \
                     "global_load_dwordx4 %3, %6, %7 offset:384\n\tglobal_load_dwordx4 %4, %6, %7 offset:512\n\tglobal_load_dwordx4 %5, %6, %7 offset:640"   \
                     : "=&v"(xq[o]), "=&v"(xq[o + 1]), "=&v"(xq[o + 2]), "=&v"(xq[o + 3]), "=&v"(xq[o + 4]), "=&v"(xq[o + 5]) : "v"(v), "s"(xp) : "memory")
            XQ6(0, vx); XQ6(6, vx2);
        }
        STAMP(1);
        GET_TAGS();
        if (role < 3) {
            VMWAIT(12);
            wg_barrier();
            STAMP(2);
        }
        f32x2 n[48];
        float stpub;
        unsigned vo = WS_VF + ((((b * 8 + j) * 8 + ((qbg >> 1) & 7)) * 4 + 2 * (qbg & 1)) * 3 + w4) * 1024 + lane * 16;
        GAS u64* gp = (GAS u64*)(ws + WS_MODG + xg * 9216 + (min(wave, 2) * 16 + row8) * 128) + sl;
        u64 g0, g1;
        UNROLL for (int hh = 0; hh < 2; ++hh) {
            if (hh == 0) asm volatile("s_waitcnt vmcnt(6)" : "+v"(xq[0]), "+v"(xq[1]), "+v"(xq[2]), "+v"(xq[3]), "+v"(xq[4]), "+v"(xq[5]) : "v"(pf0), "v"(craw) : "memory");
            else {
                asm volatile("s_waitcnt vmcnt(0)" : "+v"(xq[6]), "+v"(xq[7]), "+v"(xq[8]), "+v"(xq[9]), "+v"(xq[10]), "+v"(xq[11]) :: "memory");
                g0 = __hip_atomic_load(gp, RLX_AGENT); g1 = __hip_atomic_load(gp + 128, RLX_AGENT);
                SB();
            }
            f32x2 sm0 = {}, sm1 = {}, sq0 = {}, sq1 = {};
            UNROLL for (int i = 6 * hh; i < 6 * hh + 6; ++i)
                UNROLL for (int e = 0; e < 4; e += 2) {
                    const f32x2 a = unpack2(xq[i][e]), c = unpack2(xq[i][e + 1]);
                    n[4 * i + e] = a; n[4 * i + e + 1] = c;
                    sm0 += a; sq0 += a * a; sm1 += c; sq1 += c * c;
                }
            sm0 += sm1; sq0 += sq1;
            const f32x2 st = ln_coef(oct_sum(sm0[0] + sm0[1]), oct_sum(sq0[0] + sq0[1]));
            if (hh == 0 || (sl & 2)) stpub = (sl & 1) ? st[1] : st[0];
            UNROLL for (int i = 24 * hh; i < 24 * hh + 24; ++i) n[i] = n[i] * st[0] + st[1];
            UNROLL for (int i = 24 * hh; i < 24 * hh + 24; ++i) asm volatile("" : "+v"(n[i]));
            SB();
        }
        STAMP(4);
        if (wave < 3) {
            bool pend = (unsigned)(g0 >> 32) != tag || (unsigned)(g1 >> 32) != tag;
#pragma unroll 1
            for (int spin = 0; spin < SPIN_CAP; ++spin) {
                if (!__any(pend)) break;
                if (pend) {
                    g0 = __hip_atomic_load(gp, RLX_AGENT); g1 = __hip_atomic_load(gp + 128, RLX_AGENT);
                    pend = (unsigned)(g0 >> 32) != tag || (unsigned)(g1 >> 32) != tag;
                }
                __builtin_amdgcn_s_sleep(1);
            }
            LAS char* pm = lds + L_MOD + wave * 1024 + lane * 8;
            *(LAS f32x2*)(pm) = unpack2((unsigned)g0) + (wave > 1 ? 1.f : 0.f);
            *(LAS f32x2*)(pm + 512) = unpack2((unsigned)g1) + (wave > 0 ? 1.f : 0.f);
            lds_arrive(lds + L_CNT, lane);
        }
        if (j < 4 && sl < 4) __hip_atomic_store((GAS u64*)(ws + WS_TST + j * 32768 + (64 * qbg + 16 * wave + 8 * (sl >> 1) + row8) * 16 + (sl & 1) * 8), ((u64)tag << 32) | __float_as_uint(stpub), RLX_AGENT);
        STAMP(5);
        lds_wait(lds + L_CNT, 3);
        STAMP(6);
        {
            LAS char* pm = opaque(lds + L_MOD + sl * 32);
            LAS char* ph = opaque(lds + L_H + wave * 12288 + row8 * 128 + swz);
            f32x4 t0[6], t1[6], c0[6], c1[6];
            u32x4 o[12];
            UNROLL for (int i = 0; i < 9; ++i) {
                if (i < 6) { t0[i] = F4(pm, i * 256); c0[i] = F4(pm, i * 256 + 1536); t1[i] = F4(pm, i * 256 + 16); c1[i] = F4(pm, i * 256 + 1536 + 16); }
                if (i >= 3) { U4(ph, (i - 3) * 1024) = o[2 * (i - 3)]; }
                if (i >= 2 && i < 8) {
                    const int c = i - 2;
                    UNROLL for (int hh = 0; hh < 2; ++hh) {
                        const int k = 24 * hh + 4 * c;
                        o[2 * c + hh][0] = pack2(n[k + 0] * c0[c].xy + t0[c].xy);
                        o[2 * c + hh][1] = pack2(n[k + 1] * c0[c].zw + t0[c].zw);
                        o[2 * c + hh][2] = pack2(n[k + 2] * c1[c].xy + t1[c].xy);
                        o[2 * c + hh][3] = pack2(n[k + 3] * c1[c].zw + t1[c].zw);
                        if (hh == 0) { SB(); if (i >= 3) U4(ph, (6 + i - 3) * 1024) = o[2 * (i - 3) + 1]; }
                    }
                } else if (i >= 3) U4(ph, (6 + i - 3) * 1024) = o[2 * (i - 3) + 1];
                SB();
            }
        }
        STAMP(7);
        wg_barrier();
        STAMP(8);
        if (wave < 3) {
            u32x4 mf[12];
            ST16(empty4, vo, 0); ST16(empty4, vo, 3072);
            SB();
#pragma unroll 1
            for (int spin = 0; spin < SPIN_CAP; ++spin) {
                UNROLL for (int s = 0; s < 12; ++s) mf[s] = LDF(WS_MF + 294912 + ct * 12288, s);
                SB();
                unsigned m = 0;
                UNROLL for (int s = 0; s < 12; ++s) m = max4(m, mf[s]);
                if (!__any(m == EMPTY)) break;
                asm volatile("" ::: "memory");
            }
            lds_wait(lds + L_CNT + 12, 3);
            STAMP(22);
            STAGE1_GEMM(mfma16(hf[s][tt], mf[s], ac[tt]))
            UNROLL for (int tp = 0; tp < 2; ++tp) {
                u32x4 o = {pack2(ac[2 * tp].xy), pack2(ac[2 * tp].zw), pack2(ac[2 * tp + 1].xy), pack2(ac[2 * tp + 1].zw)};
                ST16(o, vo, tp * 3072);
            }
            if (lane < 4) count_up(ws, WS_CV + (((b * 8 + j) * 4 + ((qbg >> 2) & 3)) * 4 + lane) * 128);
            lds_arrive(lds + L_CNT + 28, lane);
            STAMP(9);
        } else {
            GAS u64* gp = (GAS u64*)(ws + WS_MODG + xg * 9216 + (48 + row8) * 128) + sl;
            u64 g0, g1, g2;
#pragma unroll 1
            for (int spin = 0; spin < SPIN_CAP; ++spin) {
                g0 = __hip_atomic_load(gp, RLX_AGENT); g1 = __hip_atomic_load(gp + 128, RLX_AGENT); g2 = __hip_atomic_load(gp + 256, RLX_AGENT);
                if (__all((unsigned)(g0 >> 32) == tag && (unsigned)(g1 >> 32) == tag && (unsigned)(g2 >> 32) == tag)) break;
                __builtin_amdgcn_s_sleep(1);
            }
            LAS char* pm = lds + L_MOD + 3072 + lane * 8;
            *(LAS f32x2*)(pm) = unpack2((unsigned)g0);
            *(LAS f32x2*)(pm + 512) = unpack2((unsigned)g1);
            *(LAS f32x2*)(pm + 1024) = unpack2((unsigned)g2);
        }
        STAMP(10);
        xr = *(const u32x4*)(x + ooff);
        lds_wait(lds + L_CNT + 4, 4);
        STAMP(12);
        const int a = (wave + (idx >> 3)) & 3;
        poll_count(ws, WS_CV + (((b * 8 + j) * 4 + a) * 4 + (xg & 3)) * 128, 12, lane == 0, pa, pb, pc_);
        STAMP(13);
        f32x4 oa[2][4][3];
#pragma unroll 1
        for (int att = 0; att < SPIN_CAP; ++att) {
            u32x4 vf[24], pf[32];
            UNROLL for (int i = 0; i < 24; ++i) vf[i] = LDF(WS_VF + ((b * 8 + j) * 8 + 2 * a) * 12288, i);
            SB();
            unsigned m = 0;
            UNROLL for (int i = 0; i < 24; ++i) m = max4(m, vf[i]);
            SB();
            if (att == 0) {
                STAMP(17);
                poll_count(ws, WS_CP + (qbg * 8 + 2 * a + lane) * 128, 4, lane < 2, pa, pb, pc_);
                lds_arrive(lds + L_CNT + 8, lane);
                STAMP(18);
            }
            const unsigned pbase = WS_PF + (qbg * 8 + 2 * a) * 16384;
            UNROLL for (int i = 0; i < PRE; ++i) pf[i] = LDF(pbase, i);
            SB();
            UNROLL for (int i = 0; i < 32; ++i) {
                const int kb = i >> 4, qt = (i >> 2) & 3, ks = i & 3;
                UNROLL for (int c = 0; c < 3; ++c) {
                    oa[kb][qt][c] = mfma16(vf[(kb * 4 + ks) * 3 + c], pf[i], ks ? oa[kb][qt][c] : (f32x4){});
                    if (c == 0 && i + PRE < 32) pf[i + PRE] = LDF(pbase, i + PRE);
                    if (c == 1) m = max4(m, pf[i]);
                    SB();
                }
            }
            if (!__any(m == EMPTY)) break;
            asm volatile("" ::: "memory");
        }
        STAMP(14);
        lds_wait(lds + L_CNT + 20, 4);
        lds_wait(lds + L_CNT + 28, 3);
        STAMP(15);
        {
            LAS char* pwt = lds + L_WT + a * 512 + r * 4;
            LAS char* pw = lds + L_H + wave * PPITCH * 64 + r * PPITCH + g * 8;
            float w0[4], w1[4];
            UNROLL for (int qt = 0; qt < 4; ++qt) { w0[qt] = *(LAS float*)(pwt + qt * 64); w1[qt] = *(LAS float*)(pwt + 256 + qt * 64); }
            UNROLL for (int qt = 0; qt < 4; ++qt)
                UNROLL for (int c = 0; c < 3; ++c) {
                    const f32x4 v = oa[0][qt][c] * w0[qt] + oa[1][qt][c] * w1[qt];
                    *(LAS u32x2*)(pw + qt * 16 * PPITCH + c * 32) = (u32x2){pack2(v.xy), pack2(v.zw)};
                }
        }
    }
    asm volatile("" :: "v"(pa), "v"(pb), "v"(pc_));
    STAMP(19);
    wg_barrier();
    STAMP(20);
    if (wave < 6) {
        u32x4 pv[4];
        LAS char* pr = lds + L_H + oq * PPITCH + oc * 16;
        UNROLL for (int k = 0; k < 4; ++k) pv[k] = U4(pr, k * PPITCH * 64);
        const f32x4 gt0 = F4(lds, L_MOD + 3072 + (48 * j + 8 * oc) * 4), gt1 = F4(lds, L_MOD + 3072 + (48 * j + 8 * oc) * 4 + 16);
        f32x2 o[4];
        UNROLL for (int e = 0; e < 4; ++e) o[e] = (unpack2(pv[0][e]) + unpack2(pv[1][e])) + (unpack2(pv[2][e]) + unpack2(pv[3][e]));
        u32x4 w = {pack2(o[0] * gt0.xy + unpack2(xr[0])), pack2(o[1] * gt0.zw + unpack2(xr[1])),
                   pack2(o[2] * gt1.xy + unpack2(xr[2])), pack2(o[3] * gt1.zw + unpack2(xr[3]))};
        { const __amdgpu_buffer_rsrc_t ro = __builtin_amdgcn_make_buffer_rsrc(out, 0, 2048 * 768, 0x00020000); __builtin_amdgcn_raw_buffer_store_b128(w, ro, ooff * 2, 0, 16); }
        if ((cid & 127) == 0 && tid == 0) put_flag(ws, WS_EPOCH + b * 4, tag);
    }
    STAMP(29); STAMPC(31);
    STAMP_OUT;
    asm volatile(".Lpf_end:");
}

extern "C" void kernel_launch(void* const* d_in, const int* in_sizes, int n_in, void* d_out, int out_size, void* d_ws, size_t ws_size, hipStream_t stream) {
    dit_fused<<<256, 512, 0, stream>>>((const u16*)d_in[0], (const u16*)d_in[1], (const u16*)d_in[2], (const u16*)d_in[3], (const u16*)d_in[4], (u16*)d_out, (char*)d_ws);
}
```

```cpp
#include <hip/hip_runtime.h>
#include <hip/hip_bf16.h>

typedef unsigned short u16;
typedef unsigned long long u64;
typedef __bf16 bf16x2 __attribute__((ext_vector_type(2)));
typedef __bf16 bf16x8 __attribute__((ext_vector_type(8)));
typedef short s16x4 __attribute__((ext_vector_type(4)));
typedef float f32x2 __attribute__((ext_vector_type(2)));
typedef float f32x4 __attribute__((ext_vector_type(4)));
typedef u64 u64x2 __attribute__((ext_vector_type(2)));
typedef u64 u64x4 __attribute__((ext_vector_type(4)));
typedef unsigned u32x2 __attribute__((ext_vector_type(2)));
typedef unsigned u32x4 __attribute__((ext_vector_type(4)));
#define GAS __attribute__((address_space(1)))
#define LAS __attribute__((address_space(3)))
#define DEV __device__ __forceinline__
#define RLX_AGENT __ATOMIC_RELAXED, __HIP_MEMORY_SCOPE_AGENT
#define SB() __builtin_amdgcn_sched_barrier(0)
#define UNROLL _Pragma("unroll")

constexpr int D = 384;
constexpr unsigned WS_EPOCH = 0;
constexpr unsigned WS_MODG = 256;
constexpr unsigned WS_F0 = WS_MODG + 8 * 9344;
constexpr unsigned WS_CNT = WS_F0 + 2 * 288 * 1024;
constexpr unsigned WS_CQ = WS_CNT, WS_CV = WS_CQ + 32 * 128, WS_CP = WS_CV + 256 * 128;
constexpr unsigned WS_TST = WS_CNT + 320 * 128;
constexpr unsigned WS_MF = WS_TST + 4 * 32768;
constexpr unsigned WS_QF = WS_MF + 2 * 294912;
constexpr unsigned WS_VF = WS_QF + 1572864;
constexpr unsigned WS_PF = WS_VF + 1572864;
constexpr unsigned WS_ML = WS_PF + 256 * 16384;
constexpr unsigned WS_END = WS_ML + 256 * 512;
constexpr int L_XK = 0;
constexpr int L_SA = 73728, L_SB = 98304;
constexpr int L_H = 98304;
constexpr int L_MOD = 147456;
constexpr int L_KST = 152064;
constexpr int L_WT = 153088;
constexpr int L_ML = 153088 + 2048;
constexpr int L_CNT = 159232;
constexpr int C_MOD = L_CNT, C_QLD = L_CNT + 4, C_ML = L_CNT + 8, C_QPUB = L_CNT + 12, C_QSEEN = L_CNT + 16, C_WT = L_CNT + 20, C_OPFREE = L_CNT + 24,
              C_VPUB = L_CNT + 28, C_H = L_CNT + 32, C_KEYS = L_CNT + 36, C_PPUB = L_CNT + 40, C_PSEEN = L_CNT + 44, C_QDONE = L_CNT + 48, C_WVO = L_CNT + 52, C_LN = L_CNT + 56;
constexpr int L_TOTAL = 159296;
constexpr int SPIN_CAP = 1 << 16, RETRY_CAP = 1 << 12;
constexpr int PPITCH = 112;
constexpr int KPRE = 2, QPRE = 3, VPRE = 9, P0 = 4, PRE = 10, WI = 15, K7 = 10;

DEV void wg_barrier() { asm volatile("s_waitcnt lgkmcnt(0)\n\ts_barrier" ::: "memory"); }
#define VMWAIT(n) asm volatile("s_waitcnt vmcnt(" #n ")" ::: "memory")
DEV unsigned pack2(float a, float b) { f32x2 f = {a, b}; return __builtin_bit_cast(unsigned, __builtin_convertvector(f, bf16x2)); }
DEV unsigned pack2(f32x2 f) { return __builtin_bit_cast(unsigned, __builtin_convertvector(f, bf16x2)); }
DEV float blo(unsigned u) { return __uint_as_float(u << 16); }
DEV float bhi(unsigned u) { return __uint_as_float(u & 0xffff0000u); }
DEV f32x2 unpack2(unsigned u) { return (f32x2){blo(u), bhi(u)}; }
DEV f32x4 mfma16(u32x4 a, u32x4 b, f32x4 c) { return __builtin_amdgcn_mfma_f32_16x16x32_bf16(__builtin_bit_cast(bf16x8, a), __builtin_bit_cast(bf16x8, b), c, 0, 0, 0); }
DEV float rows_sum(float v) {
    auto a = __builtin_amdgcn_permlane16_swap(__float_as_uint(v), __float_as_uint(v), false, false);
    v = __uint_as_float(a[0]) + __uint_as_float(a[1]);
    auto b = __builtin_amdgcn_permlane32_swap(__float_as_uint(v), __float_as_uint(v), false, false);
    return __uint_as_float(b[0]) + __uint_as_float(b[1]);
}
DEV float rows_max(float v) {
    auto a = __builtin_amdgcn_permlane16_swap(__float_as_uint(v), __float_as_uint(v), false, false);
    v = fmaxf(__uint_as_float(a[0]), __uint_as_float(a[1]));
    auto b = __builtin_amdgcn_permlane32_swap(__float_as_uint(v), __float_as_uint(v), false, false);
    return fmaxf(__uint_as_float(b[0]), __uint_as_float(b[1]));
}
DEV float oct_sum(float v) {
    v += __int_as_float(__builtin_amdgcn_mov_dpp(__float_as_int(v), 0xB1, 0xf, 0xf, true));
    v += __int_as_float(__builtin_amdgcn_mov_dpp(__float_as_int(v), 0x4E, 0xf, 0xf, true));
    v += __int_as_float(__builtin_amdgcn_mov_dpp(__float_as_int(v), 0x141, 0xf, 0xf, true));
    return v;
}
DEV f32x2 ln_coef(float sum, float sq) {
    const float mu = sum * (1.f / D);
    const float rstd = __builtin_amdgcn_rsqf(fmaxf(sq * (1.f / D) - mu * mu, 0.f) + 1e-5f);
    return (f32x2){rstd, -mu * rstd};
}
DEV void poll_flags(char* ws, unsigned off, unsigned tag, bool active) {
    GAS unsigned* p = (GAS unsigned*)(ws + off);
    bool pend = active;
#pragma unroll 1
    for (int spin = 0; spin < SPIN_CAP; ++spin) {
        if (pend) pend = __hip_atomic_load(p, RLX_AGENT) != tag;
        if (!__any(pend)) break;
        __builtin_amdgcn_s_sleep(2);
    }
    asm volatile("" ::: "memory");
}
DEV void lds_arrive(unsigned addr, unsigned n = 1u) {
    u64 ex;
    asm volatile("s_mov_b64 %0, exec\n\ts_mov_b64 exec, 1\n\tds_add_u32 %1, %2\n\ts_mov_b64 exec, %0" : "=&s"(ex) : "v"(addr), "v"(n) : "memory");
}
template <int N, int SLEEP> DEV void lds_wait_(unsigned addr, unsigned& v) {
    unsigned c, t;
    asm volatile("s_mov_b32 %1, 0x100000\n1:\n\tds_read_b32 %0, %3\n\ts_waitcnt lgkmcnt(0)\n\tv_readfirstlane_b32 %2, %0\n\ts_cmp_ge_u32 %2, %4\n\ts_cbranch_scc1 2f\n\t"
                 "s_sleep %5\n\ts_sub_u32 %1, %1, 1\n\ts_cbranch_scc0 1b\n2:" : "+v"(v), "=&s"(c), "=&s"(t) : "v"(addr), "n"(N), "n"(SLEEP) : "scc", "memory");
}
#define LWAIT(n, sleep, addr) lds_wait_<n, sleep>(addr, pc_)
#define PISSUE(x) asm volatile("global_atomic_or %0, %1, %2, %3 sc0 sc1" : "+v"(x) : "v"(off), "v"(0u), "s"(ws) : "memory")
#define PSTEP(x, d) asm volatile("s_waitcnt vmcnt(" #d ")" : "+v"(x) :: "memory"); if (__all(x >= n)) break; PISSUE(x)
template <int DEPTH> DEV void poll_count(char* ws, unsigned off, unsigned n, int lane, unsigned& pa, unsigned& pb) {
    if (lane == 0) {
        PISSUE(pa);
        if (DEPTH == 2) {
            __builtin_amdgcn_s_sleep(8); PISSUE(pb);
#pragma unroll 1
            for (int spin = 0; spin < SPIN_CAP; ++spin) { PSTEP(pa, 1); PSTEP(pb, 1); }
        } else {
#pragma unroll 1
            for (int spin = 0; spin < SPIN_CAP; ++spin) { PSTEP(pa, 0); }
        }
    }
    asm volatile("" ::: "memory");
}
template <int MASK> DEV void count_up(char* ws, unsigned off) {
    u64 ex;
    asm volatile("s_mov_b64 %0, exec\n\ts_mov_b64 exec, %4\n\tglobal_atomic_add %1, %2, %3\n\ts_mov_b64 exec, %0" : "=&s"(ex) : "v"(off), "v"(1u), "s"(ws), "n"(MASK) : "memory");
}
DEV unsigned max3u(unsigned a, unsigned b, unsigned c) { return max(max(a, b), c); }
DEV unsigned max4(unsigned m, u32x4 v) { return max3u(max3u(m, v[0], v[1]), v[2], v[3]); }
constexpr unsigned EMPTY = 0xffffffffu;
DEV void put_flag(char* ws, unsigned off, unsigned tag) { __hip_atomic_store((GAS unsigned*)(ws + off), tag, RLX_AGENT); }
DEV void glds(unsigned voff, const char* sbase, unsigned ldsdst) {
    asm volatile("s_mov_b32 m0, %2\n\ts_nop 0\n\tglobal_load_lds_dwordx4 %0, %1" :: "v"(voff), "s"(sbase), "s"(ldsdst) : "memory");
}
#define U4(p, off) (*(LAS u32x4*)((p) + (off)))
#define F4(p, off) (*(LAS f32x4*)((p) + (off)))
#define LD16(voff, soff) __builtin_amdgcn_raw_buffer_load_b128(rs, (voff), (soff), 16)
#define LDF(base, i) LD16(lane * 16 + ((i) & 3) * 1024, (base) + ((i) >> 2) * 4096)
#define ST16(v, voff, soff) __builtin_amdgcn_raw_buffer_store_b128((v), rs, (voff) + (soff), 0, 16)
#define ST8(v, voff, soff) __builtin_amdgcn_raw_buffer_store_b64((v), rs, (voff) + (soff), 0, 16)
#define WARM(voff) asm volatile("global_load_dword %0, %1, %2 sc1" : "+v"(warm_sink) : "v"(voff), "s"(ws) : "memory")
DEV u32x4 tr_frag(LAS char* p0, LAS char* p1) {
    const u32x2 l2 = __builtin_bit_cast(u32x2, __builtin_amdgcn_ds_read_tr16_b64_v4i16((LAS s16x4*)p0));
    const u32x2 h2 = __builtin_bit_cast(u32x2, __builtin_amdgcn_ds_read_tr16_b64_v4i16((LAS s16x4*)p1));
    return (u32x4){l2[0], l2[1], h2[0], h2[1]};
}
DEV LAS char* opaque(LAS char* p) { asm volatile("" : "+v"(p)); return p; }
#define FRAG(pe, po, t, s) U4(((s) & 1) ? (po) : (pe), ((t) * 12 + ((s) >> 1)) * 1024)

extern "C" __global__ __launch_bounds__(512, 2) void dit_fused(const u16* x_, const u16* cvec_, const u16* wqkv_, const u16* wo_, const u16* wmod_, u16* out_, char* ws_) {
    u64x4 ka; u64x2 kb_; u64 kc;
    asm volatile("s_load_dwordx8 %0, %3, 0x0\n\ts_load_dwordx4 %1, %3, 0x20\n\ts_load_dwordx2 %2, %3, 0x30"
                 : "=&s"(ka), "=&s"(kb_), "=&s"(kc) : "s"(__builtin_amdgcn_kernarg_segment_ptr()) : "memory");
    __shared__ __attribute__((aligned(16))) char smem[L_TOTAL];
    LAS char* lds = (LAS char*)smem;
    const unsigned lds0 = (unsigned)(size_t)lds;
    const int tid = threadIdx.x ^ 256, lane = tid & 63;
    const int wave = __builtin_amdgcn_readfirstlane(tid >> 6);
    const int bid = blockIdx.x, xg = bid & 7, idx = bid >> 3;
    unsigned pf0;
    {
        u64 pc; unsigned len;
        asm volatile("s_getpc_b64 %0\n.Lpf_here:\n\ts_mov_b32 %1, .Lpf_end-.Lpf_here" : "=s"(pc), "=s"(len));
        unsigned o0 = min((unsigned)(((idx & 3) * 8 + wave) * 8 + (lane >> 3)) * 128u, len);
        asm volatile("global_load_dword %0, %1, %2" : "=&v"(pf0) : "v"(o0), "s"(pc) : "memory");
    }
    if (tid < 16) *(LAS unsigned*)(lds + L_CNT + tid * 4) = 0u;

    const int b = xg >> 2;
    const int qbg = b * 16 + (xg & 3) * 4 + (idx >> 3);
    const int j = idx & 7;
    const int cid = qbg * 8 + j;
    const int r = lane & 15, g = lane >> 4, row8 = lane >> 3, sl = lane & 7;
    const unsigned swz = (unsigned)(sl ^ row8) * 16u;
    const int w4 = wave & 3;
    const int is0 = (0x001ffff8u >> idx) & 1, is1 = (0x3fe00000u >> idx) & 1, is2 = (bid < 18) | (idx == 30) | (bid >= 252);
    const int role = 3 - 3 * is0 - 2 * is1 - is2;
    const int slice = idx < 3 ? bid : (xg ^ 4) + 8 * (idx & 1);
    const int ti = idx - 21 + 18 * is0;
    const int tq = (ti * (86 - 43 * is0)) >> 8;
    const int s0 = 3 * (xg >> 1) + tq;
    const int cb = ((xg & 1) - tq) * (3 + 3 * is0) + ti;
    const int strip = (4 - 2 * is0) * cb + w4;
    const bool s0w = is0 ? w4 < 2 : role < 3;
    unsigned va0, vb0, vb1, vb2, vx, ma, mb, bstep;
    {
        const int rg = wave >> 1, c0 = 3 * (wave & 1);
        const int ldb = is0 ? 2304 : is1 ? 768 : 4608;
        va0 = (32 * s0 + 16 * (rg & 1) + 4 * (rg >> 1) + 8 * (row8 >> 2) + (row8 & 3)) * 2304 + is1 * 1536 + c0 * 128 + swz;
        if (role == 2) va0 = min(tid & 255, 191) * 8;
        const unsigned step = is0 ? 128 : 8 * ldb;
        vb0 = (is0 ? 768 + (32 * cb + 8 * rg) * 2304 + c0 * 128 : (is1 ? cb : slice) * 128 + 48 * wave * ldb) + row8 * ldb + swz;
        vb1 = (vb0 + step) ^ (is0 ? 0u : 64u);
        vb2 = vb0 + 2 * step;
        bstep = 2 * step;
        ma = lds0 + L_SA + (rg * 6 + c0) * 1024;
        mb = lds0 + L_SB + (is0 ? rg * 6 + c0 : wave * 6) * 1024;
        vx = (64 * qbg + 16 * w4 + row8) * 768 + sl * 16;
    }
    asm volatile("s_waitcnt lgkmcnt(0)" : "+v"(va0), "+v"(vb0), "+v"(vb1), "+v"(vb2), "+v"(vx), "+s"(ma), "+s"(mb), "+s"(bstep), "+s"(ka), "+s"(kb_), "+s"(kc)
                 : "s"(role) : "memory");
    const u16* const x = (const u16*)(GAS const u16*)ka[0]; const u16* const cvec = (const u16*)(GAS const u16*)ka[1];
    const u16* const wqkv = (const u16*)(GAS const u16*)ka[2]; const u16* const wo = (const u16*)(GAS const u16*)ka[3];
    const u16* const wmod = (const u16*)(GAS const u16*)kb_[0]; u16* const out = (u16*)(GAS u16*)kb_[1]; char* const ws = (char*)(GAS char*)kc;

    u32x2 craw = {};
    {
        const u16* bb = is0 ? wqkv : is1 ? wo : wmod;
#define G1(v, base) "s_nop 0\n\tglobal_load_lds_dwordx4 " v ", " base "\n\ts_addk_i32 m0, 0x400\n\t"
        asm volatile("s_cmp_eq_u32 %9, 3\n\ts_cbranch_scc1 9f\n\t"
                     "s_cmp_eq_u32 %9, 2\n\ts_cbranch_scc0 1f\n\t"
                     "global_load_dwordx2 %0, %1, %10\n\ts_branch 2f\n"
                     "1:\n\ts_mov_b32 m0, %5\n\t"
                     G1("%1", "%11") "v_add_u32 %1, 0x80, %1\n\tglobal_load_lds_dwordx4 %1, %11\n\ts_addk_i32 m0, 0x400\n\t"
                     "v_add_u32 %1, 0x80, %1\n\tglobal_load_lds_dwordx4 %1, %11\n"
                     "2:\n\ts_mov_b32 m0, %6\n\t"
                     G1("%2", "%8") G1("%3", "%8") G1("%4", "%8")
                     "s_cmp_eq_u32 %9, 0\n\ts_cbranch_scc1 9f\n\t"
                     "v_add_u32 %3, %7, %3\n\tglobal_load_lds_dwordx4 %3, %8\n\ts_addk_i32 m0, 0x400\n\t"
                     "v_add_u32 %4, %7, %4\n\tglobal_load_lds_dwordx4 %4, %8\n\ts_addk_i32 m0, 0x400\n\t"
                     "v_add_u32 %3, %7, %3\n\tglobal_load_lds_dwordx4 %3, %8\n"
                     "9:"
                     : "+v"(craw), "+v"(va0), "+v"(vb0), "+v"(vb1), "+v"(vb2) : "s"(ma), "s"(mb), "s"(bstep), "s"(bb), "s"(role), "s"(cvec), "s"(wqkv) : "scc", "memory");
    }
    u64 ep;
    asm volatile("s_load_dwordx2 %0, %1, 0x0" : "=s"(ep) : "s"(ws) : "memory");
    const __amdgpu_buffer_rsrc_t rs = __builtin_amdgcn_make_buffer_rsrc(ws, 0, WS_END, 0x00020000);
    const u32x4 empty4 = {EMPTY, EMPTY, EMPTY, EMPTY};
    unsigned pa = 0, pb = 0, pc_ = 0;
    const unsigned mo = WS_MF + role * 294912 + (strip * 12 + s0) * 1024 + lane * 16;
    const unsigned fo = WS_F0 + (lane * 576 + (role * 24 + strip) * 12 + s0) * 128;
    if (wave > 3 && role < 2 && s0w) {
        ST16(empty4, mo, 0);
    }
    const int lpe = (r >> 3) * 6144 + (r & 7) * 128 + ((g ^ (r & 7)) * 16), lpo = lpe ^ 64;
    const unsigned vk = row8 * 768 + swz;
    const char* kp = (const char*)x + (b * 1024 + 128 * j) * 768;
#define KEY_DMA(rg0, n) do { _Pragma("unroll 1") for (int rg = (rg0); rg < (rg0) + (n); ++rg) { \
        UNROLL for (int i = 0; i < 6; ++i) glds(vk, kp + rg * 6144 + i * 128, lds0 + L_XK + (rg * 6 + i) * 1024); } } while (0)
    unsigned tag0, tag1, tag;
#define GET_TAGS() do { u64 e_; asm volatile("s_waitcnt lgkmcnt(0)\n\ts_mov_b64 %0, %1" : "=s"(e_) : "s"(ep)); \
        tag0 = (unsigned)e_ + 1u; tag1 = (unsigned)(e_ >> 32) + 1u; tag = b ? tag1 : tag0; } while (0)
    const int ct = 3 * j + w4;
#define STAGE1_GEMM(MBASE, HPRE, GATE, GATE2, MFMA_EXPR)                                                            \
        f32x4 ac[4];                                                                                   \
        LAS char* pe = opaque(lds + L_H + lpe); LAS char* po = opaque(lds + L_H + lpo);                \
        _Pragma("unroll 1") for (int att = 0; att < RETRY_CAP; ++att) {                                \
            u32x4 mf[12], hf[12][4];                                                                   \
            UNROLL for (int s = 0; s < 12; ++s) { mf[s] = LDF(MBASE, s); SB(); }                       \
            GATE;                                                                                      \
            UNROLL for (int s = 0; s < HPRE; ++s)                                                      \
                UNROLL for (int tt = 0; tt < 4; ++tt) { hf[s][tt] = FRAG(pe, po, tt, s); SB(); }       \
            GATE2;                                                                                     \
            unsigned m = 0;                                                                            \
            UNROLL for (int s = 0; s < 12; ++s) {                                                      \
                UNROLL for (int tt = 0; tt < 4; ++tt) {                                                \
                    const f32x4 cin = s ? ac[tt] : (f32x4){};                                          \
                    ac[tt] = MFMA_EXPR;                                                                \
                    if (s + HPRE < 12) hf[s + HPRE][tt] = FRAG(pe, po, tt, s + HPRE);                  \
                    if (tt == 0) m = max(max(m, mf[s][0]), mf[s][1]);                                  \
                    if (tt == 1) m = max(max(m, mf[s][2]), mf[s][3]);                                  \
                    SB();                                                                              \
                }                                                                                      \
            }                                                                                          \
            if (__builtin_expect(!__any(m == EMPTY), 1)) break;                                                             \
            asm volatile("" ::: "memory");                                                             \
        }
    unsigned warm_sink = 0;
    const int oq = (tid * 43691) >> 18, oc = tid - 6 * oq;
    const int ooff = (64 * qbg + oq) * D + 48 * j + 8 * oc;
    u32x4 xr = {};
    asm volatile("s_barrier" ::: "memory");

    if (wave > 3) {
        GET_TAGS();
        if (wave == 7) lds_arrive(lds0 + C_H);
        if (role == 2) {
            asm volatile("s_waitcnt vmcnt(6)" : "+v"(craw) :: "memory");
            u32x2 o;
            UNROLL for (int e = 0; e < 2; ++e) {
                float a = blo(craw[e]), c = bhi(craw[e]);
                a = a * __builtin_amdgcn_rcpf(1.f + __builtin_amdgcn_exp2f(-1.4426950408889634f * a));
                c = c * __builtin_amdgcn_rcpf(1.f + __builtin_amdgcn_exp2f(-1.4426950408889634f * c));
                o[e] = pack2(a, c);
            }
            const int t = tid - 256, row = t >= 96, q4 = t - 96 * row, ch = q4 >> 1;
            if (t < 192) *(LAS u32x2*)(lds + L_SA + (ch >> 3) * 1024 + row * 128 + (((ch & 7) ^ row) * 16) + (q4 & 1) * 8) = o;
        }
        if (role < 3) {
            VMWAIT(0);
            wg_barrier();
          if (s0w) {
            u32x4 a0[12], a1[12], bf[12];
            LAS char* pe = opaque(lds + L_SA + lpe); LAS char* po = opaque(lds + L_SA + lpo);
            f32x4 lo = {}, hi = {};
#define S0_LOOP(BLOAD)                                                                                 \
            UNROLL for (int s = 0; s < 16; ++s) {                                                      \
                if (s < 12) { BLOAD; a0[s] = FRAG(pe, po, 0, s); a1[s] = FRAG(pe, po, 1, s); }         \
                if (s >= 4) { lo = mfma16(a0[s - 4], bf[s - 4], lo); hi = mfma16(a1[s - 4], bf[s - 4], hi); } \
                SB();                                                                                  \
            }
            if (role == 0) {
                LAS char* qe = opaque(lds + L_SB + w4 * 12288 + lpe); LAS char* qo = opaque(lds + L_SB + w4 * 12288 + lpo);
                S0_LOOP(bf[s] = FRAG(qe, qo, 0, s))
            } else {
                const int q = (lane >> 2) & 3, p = lane & 3;
                LAS char* t0 = opaque(lds + L_SB + g * 1024 + q * 128 + (((2 * w4 + (p >> 1)) ^ q ^ (4 * (g & 1))) * 16) + 8 * (p & 1));
                LAS char* t1 = opaque(lds + L_SB + g * 1024 + (4 + q) * 128 + (((2 * w4 + (p >> 1)) ^ (4 + q) ^ (4 * (g & 1))) * 16) + 8 * (p & 1));
                if (role == 1) { S0_LOOP(bf[s] = tr_frag(t0 + s * 4096, t1 + s * 4096)) }
                else {
                    UNROLL for (int s = 0; s < 17; ++s) {
                        if (s < 12) { bf[s] = tr_frag(t0 + s * 4096, t1 + s * 4096); a0[s] = FRAG(pe, po, 0, s); }
                        if (s >= 5) lo = mfma16(a0[s - 5], bf[s - 5], lo);
                        SB();
                    }
                }
            }
            if (role < 2) {
                u32x4 o = {pack2(lo.xy), pack2(lo.zw), pack2(hi.xy), pack2(hi.zw)};
                ST16(o, mo, 0);
                asm volatile("" ::: "memory");
                if (lane < 8) put_flag(ws, fo, lane < 4 ? tag0 : tag1);
            } else {
                const unsigned u = pack2(lo.xy);
                const unsigned u0 = __shfl(u, 2 * sl), u1 = __shfl(u, 2 * sl + 1);
                const unsigned pay = lane < 32 ? ((u0 & 0xffffu) | (u1 << 16)) : ((u0 >> 16) | (u1 & 0xffff0000u));
                __hip_atomic_store((GAS u64*)(ws + WS_MODG + row8 * 9344 + (4 * slice + w4) * 128 + sl * 8), ((u64)(lane < 32 ? tag0 : tag1) << 32) | pay, RLX_AGENT);
            }
          }
            lds_arrive(lds0 + C_OPFREE);
        }
        unsigned qo = WS_QF + (48 * qbg + (ct >> 1)) * 1024 + (g & 1) * 12288 + ((2 * (ct & 1) + (g >> 1)) * 16 + r) * 16;
        unsigned po_ = WS_PF + (cid * 4 + w4) * 4096 + lane * 16;
        const unsigned mlo = WS_ML + cid * 512 + w4 * 128 + r * 8;
        asm volatile("" : "+v"(qo), "+v"(po_));
#define P_CLEAR() do { UNROLL for (int tp = 0; tp < 4; ++tp) ST16(empty4, po_, tp * 1024); ST8(empty4.xy, mlo, 0); SB(); } while (0)
        if (bid == 249 && wave == 4) {
            UNROLL for (int i = 0; i < 5; ++i) put_flag(ws, WS_CNT + (i * 64 + lane) * 128, 0u);
            VMWAIT(0);
            if (lane < 8) __hip_atomic_store((GAS u64*)(ws + WS_MODG + lane * 9344 + 72 * 128), (u64)(lane < 4 ? tag0 : tag1) << 32, RLX_AGENT);
        }
        if (role == 3) {
            __builtin_amdgcn_s_sleep(10);
            KEY_DMA(4 * w4, 4);
            VMWAIT(0);
            lds_arrive(lds0 + C_KEYS);
        }
        if (wave < 7) {
            ST16(empty4, qo, 0); ST16(empty4, qo, 24576);
            LWAIT(4, 2, lds0 + C_LN);
            poll_flags(ws, WS_F0 + (xg * 576 + ct * 12 + lane) * 128, tag, lane < 12);
            VMWAIT(0);
            asm volatile("" :: "v"(pf0));
            f32x4 qs;
            STAGE1_GEMM(WS_MF + ct * 12288, QPRE, { LWAIT(3, 1, lds0 + C_MOD); P_CLEAR(); LWAIT(5, 1, lds0 + C_H); }, { qs = F4(lds, L_MOD + 1536 + (16 * ct + 4 * g) * 4); SB(); }, mfma16(mf[s], hf[s][tt], cin))
            lds_arrive(lds0 + C_QDONE);
            qs *= 1.4426950408889634f * 0.05103103630798288f;
            UNROLL for (int tt = 0; tt < 4; tt += 2) {
                const f32x4 v0 = ac[tt] * qs, v1 = ac[tt + 1] * qs;
                auto r0 = __builtin_amdgcn_permlane16_swap(pack2(v0.xy), pack2(v1.xy), false, false);
                auto r1 = __builtin_amdgcn_permlane16_swap(pack2(v0.zw), pack2(v1.zw), false, false);
                u32x4 o = {r0[0], r1[0], r0[1], r1[1]};
                ST16(o, qo, tt * 12288);
            }
            count_up<1>(ws, WS_CQ + qbg * 128);
            lds_arrive(lds0 + C_QPUB);
        } else {
            if (role < 3) {
                KEY_DMA(0, K7);
                VMWAIT(0);
                lds_arrive(lds0 + C_KEYS, 4u);
            }
            asm volatile("" :: "v"(pf0));
            LWAIT(3, 1, lds0 + C_MOD);
            P_CLEAR();
            poll_flags(ws, WS_F0 + (xg * 576 + 288 + 36 * j + lane) * 128, tag, lane < 36);
            VMWAIT(0);
            lds_arrive(lds0 + C_WVO);
        }
        LAS char* pe0 = opaque(lds + L_XK + lpe); LAS char* po0 = opaque(lds + L_XK + lpo);
        LAS char* pe1 = opaque(lds + L_XK + 49152 + lpe); LAS char* po1 = opaque(lds + L_XK + 49152 + lpo);
        LAS char* pst = opaque(lds + L_KST + g * 16);
        f32x4 krs[8], kmu[8], sa[8];
        const u32x4 ones = {0x3f803f80u, 0x3f803f80u, 0x3f803f80u, 0x3f803f80u};
        const unsigned qbase = WS_QF + (4 * qbg + w4) * 12288;
#define KF(s, t) ((t) < 4 ? FRAG(pe0, po0, (t), (s)) : FRAG(pe1, po1, (t) - 4, (s)))
        if (wave == 7) {
            LWAIT(3, 1, lds0 + C_QPUB);
            poll_count<2>(ws, WS_CQ + qbg * 128, 24, lane, pa, pb);
            lds_arrive(lds0 + C_QSEEN);
        } else {
            VMWAIT(0);
            WARM(qo); WARM(qo + 24576);
            LWAIT(1, 0, lds0 + C_QSEEN);
        }
#pragma unroll 1
        for (int att = 0; att < RETRY_CAP; ++att) {
            u32x4 kf[12][8], qf[12];
            UNROLL for (int s = 0; s < 12; ++s) { qf[s] = LDF(qbase, s); SB(); }
            lds_arrive(lds0 + C_QLD);
            LWAIT(5, 0, lds0 + C_KEYS);
            UNROLL for (int s = 0; s < KPRE; ++s)
                UNROLL for (int t = 0; t < 8; ++t) { kf[s][t] = KF(s, t); SB(); }
            f32x4 rq = {};
            unsigned m = 0;
            UNROLL for (int t = 0; t < 8; ++t) sa[t] = (f32x4){};
            UNROLL for (int s = 0; s < 12; ++s) {
                rq = mfma16(ones, qf[s], rq);
                SB();
                UNROLL for (int t = 0; t < 8; ++t) {
                    sa[t] = mfma16(kf[s][t], qf[s], sa[t]);
                    if (s + KPRE < 12) kf[s + KPRE][t] = KF(s + KPRE, t);
                    else if (s == 10) { kmu[t] = F4(pst, 512 + t * 64); krs[t] = F4(pst, t * 64); }
                    else if (s == 11 && t > 0) { kmu[t - 1][0] *= rq[0]; kmu[t - 1][1] *= rq[0]; }
                    if (t == 0) m = max(max(m, qf[s][0]), qf[s][1]);
                    if (t == 1) m = max(max(m, qf[s][2]), qf[s][3]);
                    SB();
                }
            }
            kmu[7][0] *= rq[0]; kmu[7][1] *= rq[0];
            UNROLL for (int t = 0; t < 8; ++t) { kmu[t][2] *= rq[0]; kmu[t][3] *= rq[0]; }
            if (__builtin_expect(!__any(m == EMPTY), 1)) break;
            asm volatile("" ::: "memory");
        }
        float mx = -3.0e38f;
        UNROLL for (int t = 0; t < 8; ++t) {
            sa[t] = sa[t] * krs[t] + kmu[t];
            mx = fmaxf(fmaxf(fmaxf(mx, sa[t][0]), sa[t][1]), fmaxf(sa[t][2], sa[t][3]));
        }
        mx = rows_max(mx);
        f32x4 ls = {};
        UNROLL for (int tp = 0; tp < 4; ++tp) {
            UNROLL for (int t = 2 * tp; t < 2 * tp + 2; ++t) {
                sa[t] -= mx;
                UNROLL for (int e = 0; e < 4; ++e) sa[t][e] = __builtin_amdgcn_exp2f(sa[t][e]);
                ls += sa[t];
            }
            u32x4 o = {pack2(sa[2 * tp].xy), pack2(sa[2 * tp].zw), pack2(sa[2 * tp + 1].xy), pack2(sa[2 * tp + 1].zw)};
            ST16(o, po_, tp * 1024);
        }
        const float l = rows_sum((ls[0] + ls[1]) + (ls[2] + ls[3]));
        const u32x2 ml = {__float_as_uint(mx), __float_as_uint(l)};
        if (g == 0) ST8(ml, mlo, 0);
        count_up<1>(ws, WS_CP + qbg * 128);
        lds_arrive(lds0 + C_PPUB);
        VMWAIT(0);
        { const unsigned wo_ = WS_PF + (cid * 4 + w4) * 4096 + lane * 64; WARM(wo_); WARM(mlo); }
        if (wave < 6) xr = *(const u32x4*)(x + ooff);
        if (cid == 0 && wave == 7) {
            GAS unsigned* p = (GAS unsigned*)(ws + WS_CQ + (lane & 31) * 128);
#pragma unroll 1
            for (int spin = 0; spin < SPIN_CAP; ++spin) { if (__all(__hip_atomic_load(p, RLX_AGENT) >= 24u)) break; __builtin_amdgcn_s_sleep(8); }
            if (lane < 2) put_flag(ws, WS_EPOCH + lane * 4, lane ? tag1 : tag0);
        }
        LWAIT(4, 1, lds0 + C_ML);
        const u32x2 m0 = *(LAS u32x2*)(lds + L_ML + g * 512 + (16 * w4 + r) * 8), m1 = *(LAS u32x2*)(lds + L_ML + 2048 + g * 512 + (16 * w4 + r) * 8);
        const float gm = rows_max(fmaxf(__uint_as_float(m0[0]), __uint_as_float(m1[0])));
        const float w0 = __builtin_amdgcn_exp2f(__uint_as_float(m0[0]) - gm), w1 = __builtin_amdgcn_exp2f(__uint_as_float(m1[0]) - gm);
        const float inv = __builtin_amdgcn_rcpf(rows_sum(w0 * __uint_as_float(m0[1]) + w1 * __uint_as_float(m1[1])));
        *(LAS float*)(lds + L_WT + g * 256 + (16 * w4 + r) * 4) = w0 * inv;
        *(LAS float*)(lds + L_WT + 1024 + g * 256 + (16 * w4 + r) * 4) = w1 * inv;
        lds_arrive(lds0 + C_WT);
        VMWAIT(0);
        asm volatile("" :: "v"(warm_sink));
    } else {
        u32x4 xq[12];
        if (role == 2) { VMWAIT(0); wg_barrier(); __builtin_amdgcn_s_sleep(20); }
        {
            const u16* xp = x;
            const unsigned vx2 = vx + 6144;
#define XQ6(o, v) asm volatile("global_load_dwordx4 %0, %6, %7\n\tglobal_load_dwordx4 %1, %6, %7 offset:128\n\tglobal_load_dwordx4 %2, %6, %7 offset:256\n\t" \
                     "global_load_dwordx4 %3, %6, %7 offset:384\n\tglobal_load_dwordx4 %4, %6, %7 offset:512\n\tglobal_load_dwordx4 %5, %6, %7 offset:640"   \
                     : "=&v"(xq[o]), "=&v"(xq[o + 1]), "=&v"(xq[o + 2]), "=&v"(xq[o + 3]), "=&v"(xq[o + 4]), "=&v"(xq[o + 5]) : "v"(v), "s"(xp) : "memory")
            XQ6(0, vx); XQ6(6, vx2);
        }
        GET_TAGS();
        if (role < 2) {
            VMWAIT(12);
            wg_barrier();
        }
        f32x2 n[48];
        float stpub;
        u32x2 tpub;
        unsigned vo = WS_VF + ((((b * 8 + j) * 8 + ((qbg >> 1) & 7)) * 4 + 2 * (qbg & 1)) * 3 + w4) * 1024 + lane * 16;
        GAS u64* gp = (GAS u64*)(ws + WS_MODG + xg * 9344 + row8 * 128) + sl;
        UNROLL for (int hh = 0; hh < 2; ++hh) {
            if (hh == 0) asm volatile("s_waitcnt vmcnt(6)" : "+v"(xq[0]), "+v"(xq[1]), "+v"(xq[2]), "+v"(xq[3]), "+v"(xq[4]), "+v"(xq[5]) : "v"(pf0), "v"(craw) : "memory");
            else {
                asm volatile("s_waitcnt vmcnt(0)" : "+v"(xq[6]), "+v"(xq[7]), "+v"(xq[8]), "+v"(xq[9]), "+v"(xq[10]), "+v"(xq[11]) :: "memory");
            }
            f32x2 sm0 = {}, sm1 = {}, sq0 = {}, sq1 = {};
            UNROLL for (int i = 6 * hh; i < 6 * hh + 6; ++i)
                UNROLL for (int e = 0; e < 4; e += 2) {
                    const f32x2 a = unpack2(xq[i][e]), c = unpack2(xq[i][e + 1]);
                    n[4 * i + e] = a; n[4 * i + e + 1] = c;
                    sm0 += a; sq0 += a * a; sm1 += c; sq1 += c * c;
                }
            sm0 += sm1; sq0 += sq1;
            const f32x2 st = ln_coef(oct_sum(sm0[0] + sm0[1]), oct_sum(sq0[0] + sq0[1]));
            if (hh == 0 || (sl & 2)) stpub = (sl & 1) ? st[1] : st[0];
            if (hh == 1) {
                tpub = (u32x2){__float_as_uint(stpub), tag};
                ST8(tpub, j < 4 && sl < 4 ? WS_TST + j * 32768 + (64 * qbg + 16 * wave + 8 * (sl >> 1) + row8) * 16 + (sl & 1) * 8 : WS_END, 0);
                SB();
            }
            UNROLL for (int i = 24 * hh; i < 24 * hh + 24; ++i) n[i] = n[i] * st[0] + st[1];
            UNROLL for (int i = 24 * hh; i < 24 * hh + 24; ++i) asm volatile("" : "+v"(n[i]));
            SB();
        }
        lds_arrive(lds0 + C_LN);
        if (wave == 3) {
            u64 gm[6];
            UNROLL for (int i = 0; i < 6; ++i) gm[i] = (u64)~tag << 32;
            unsigned zt = lane ? tag : ~tag;
#pragma unroll 1
            for (int spin = 0; spin < SPIN_CAP; ++spin) {
                if (zt != tag) zt = __hip_atomic_load((GAS unsigned*)(ws + WS_MODG + xg * 9344 + 72 * 128 + 4), RLX_AGENT);
                UNROLL for (int i = 0; i < 6; ++i) if ((unsigned)(gm[i] >> 32) != tag) gm[i] = __hip_atomic_load(gp + 128 * i, RLX_AGENT);
                bool pend = zt != tag;
                UNROLL for (int i = 0; i < 6; ++i) pend |= (unsigned)(gm[i] >> 32) != tag;
                if (!__any(pend)) break;
                __builtin_amdgcn_s_sleep(1);
            }
            LAS char* pm = lds + L_MOD + lane * 8;
            UNROLL for (int i = 0; i < 6; ++i) *(LAS f32x2*)(pm + 512 * i) = unpack2((unsigned)gm[i]) + (i >= 3 ? 1.f : 0.f);
            lds_arrive(lds0 + C_MOD, 3u);
        }
        if (role < 3) LWAIT(4, 1, lds0 + C_OPFREE);
        LWAIT(3, 0, lds0 + C_MOD);
        {
            LAS char* pm = opaque(lds + L_MOD + sl * 32);
            LAS char* ph = opaque(lds + L_H + wave * 12288 + row8 * 128 + swz);
            f32x4 t0[6], t1[6], c0[6], c1[6];
            u32x4 o[12];
            UNROLL for (int i = 0; i < 9; ++i) {
                if (i < 6) { t0[i] = F4(pm, i * 256); c0[i] = F4(pm, i * 256 + 1536); t1[i] = F4(pm, i * 256 + 16); c1[i] = F4(pm, i * 256 + 1536 + 16); }
                if (i >= 3) { U4(ph, (i - 3) * 1024) = o[2 * (i - 3)]; }
                if (i >= 2 && i < 8) {
                    const int c = i - 2;
                    UNROLL for (int hh = 0; hh < 2; ++hh) {
                        const int k = 24 * hh + 4 * c;
                        o[2 * c + hh][0] = pack2(n[k + 0] * c0[c].xy + t0[c].xy);
                        o[2 * c + hh][1] = pack2(n[k + 1] * c0[c].zw + t0[c].zw);
                        o[2 * c + hh][2] = pack2(n[k + 2] * c1[c].xy + t1[c].xy);
                        o[2 * c + hh][3] = pack2(n[k + 3] * c1[c].zw + t1[c].zw);
                        if (hh == 0) { SB(); if (i >= 3) U4(ph, (6 + i - 3) * 1024) = o[2 * (i - 3) + 1]; }
                    }
                } else if (i >= 3) U4(ph, (6 + i - 3) * 1024) = o[2 * (i - 3) + 1];
                SB();
            }
        }
        lds_arrive(lds0 + C_H);
        asm volatile("" :: "v"(tpub));
        if (wave < 3) {
            ST16(empty4, vo, 0); ST16(empty4, vo, 3072);
            LWAIT(1, 1, lds0 + C_WVO);
            STAGE1_GEMM(WS_MF + 294912 + ct * 12288, VPRE, { LWAIT(5, 1, lds0 + C_H); }, { LWAIT(3, 1, lds0 + C_QDONE); }, mfma16(hf[s][tt], mf[s], cin))
            VMWAIT(0);
            UNROLL for (int tp = 0; tp < 2; ++tp) {
                u32x4 o = {pack2(ac[2 * tp].xy), pack2(ac[2 * tp].zw), pack2(ac[2 * tp + 1].xy), pack2(ac[2 * tp + 1].zw)};
                ST16(o, vo, tp * 3072);
            }
            count_up<15>(ws, WS_CV + (((b * 8 + j) * 4 + ((qbg >> 2) & 3)) * 4 + lane) * 128);
            lds_arrive(lds0 + C_VPUB);
        } else {
            if (role < 3) KEY_DMA(K7, 16 - K7);
            GAS u64* gp = (GAS u64*)(ws + WS_MODG + xg * 9344 + (48 + row8) * 128) + sl;
            GAS u64* tp = (GAS u64*)(ws + WS_TST + (xg & 3) * 32768 + (b * 1024 + 128 * j) * 16) + lane;
            u64 gv[7];
#pragma unroll 1
            for (int spin = 0; spin < SPIN_CAP; ++spin) {
                bool ok = true;
                UNROLL for (int i = 0; i < 3; ++i) gv[i] = __hip_atomic_load(gp + 128 * i, RLX_AGENT);
                UNROLL for (int i = 0; i < 4; ++i) gv[3 + i] = __hip_atomic_load(tp + 64 * i, RLX_AGENT);
                UNROLL for (int i = 0; i < 7; ++i) ok &= (unsigned)(gv[i] >> 32) == tag;
                if (__all(ok)) break;
                __builtin_amdgcn_s_sleep(2);
            }
            LAS char* pm = lds + L_MOD + 3072 + lane * 8;
            LAS char* pk = lds + L_KST + (lane & 1) * 512 + (lane >> 1) * 4;
            UNROLL for (int i = 0; i < 3; ++i) *(LAS f32x2*)(pm + 512 * i) = unpack2((unsigned)gv[i]);
            UNROLL for (int i = 0; i < 4; ++i) *(LAS unsigned*)(pk + 128 * i) = (unsigned)gv[3 + i];
            VMWAIT(0);
            lds_arrive(lds0 + C_KEYS);
        }
        xr = *(const u32x4*)(x + ooff);
        const int a = (wave + (idx >> 3)) & 3;
        LWAIT(3, 1, lds0 + C_VPUB);
        LWAIT(4, 1, lds0 + C_QLD);
        poll_count<1>(ws, WS_CV + (((b * 8 + j) * 4 + a) * 4 + (xg & 3)) * 128, 12, lane, pa, pb);
        unsigned mlnew = 1u;
        LAS char* pwt = opaque(lds + L_WT + a * 512 + r * 4);
        LAS char* pw = opaque(lds + L_H + wave * PPITCH * 64 + r * PPITCH + g * 8);
#pragma unroll 1
        for (int att = 0; att < RETRY_CAP; ++att) {
            u32x4 vf[24], pf[32];
            f32x4 oa[2][4][3];
            float w0[4], w1[4];
            UNROLL for (int i = 0; i < 24; ++i) { vf[i] = LDF(WS_VF + ((b * 8 + j) * 8 + 2 * a) * 12288, i); SB(); }
            unsigned m = 0;
            UNROLL for (int i = 0; i < 24; ++i) m = max4(m, vf[i]);
            SB();
            const unsigned pbase = WS_PF + (qbg * 8 + 2 * a) * 16384;
            if (att == 0) {
                if (wave == 0) {
                    LWAIT(4, 1, lds0 + C_PPUB);
                    poll_count<2>(ws, WS_CP + qbg * 128, 32, lane, pa, pb);
                    lds_arrive(lds0 + C_PSEEN);
                } else LWAIT(1, 0, lds0 + C_PSEEN);
            }
            u32x4 ml = LD16(lane * 16, WS_ML + (qbg * 8 + 2 * a) * 512);
            SB();
            UNROLL for (int i = 0; i < P0; ++i) { pf[i] = LDF(pbase, i); SB(); }
            {
                const unsigned go = !__any(max4(0u, ml) == EMPTY) || att + 1 == RETRY_CAP;
                m = max4(m, ml);
                U4(lds, L_ML + a * 1024 + lane * 16) = ml;
                lds_arrive(lds0 + C_ML, go & mlnew);
                mlnew &= ~go;
            }
            SB();
            UNROLL for (int i = 0; i < 36; ++i) {
                const int kb = i >> 4, qt = (i >> 2) & 3, ks = i & 3;
                if (i == WI) {
                    LWAIT(4, 0, mlnew ? lds0 + C_PPUB : lds0 + C_WT);
                    UNROLL for (int q = 0; q < 4; ++q) { w0[q] = *(LAS float*)(pwt + q * 64); w1[q] = *(LAS float*)(pwt + 256 + q * 64); }
                    SB();
                }
                UNROLL for (int c = 0; c < 3; ++c) {
                    if (i < 32) {
                        oa[kb][qt][c] = mfma16(vf[(kb * 4 + ks) * 3 + c], pf[i], ks ? oa[kb][qt][c] : (f32x4){});
                        if (i < PRE - P0) { if (c < 2) pf[P0 + 2 * i + c] = LDF(pbase, P0 + 2 * i + c); }
                        else if (c == 0 && i + PRE < 32) pf[i + PRE] = LDF(pbase, i + PRE);
                        if (c == 2) m = max4(m, pf[i]);
                    }
                    if (i >= 17 && i < 29 && c == 1) { const int u = i - 17; oa[0][u / 3][u % 3] *= w0[u / 3]; asm volatile("" : "+v"(oa[0][u / 3][u % 3])); }
                    if (i >= 21 && ks > 0 && c == 0) {
                        const int fq = ((i - 4) >> 2) & 3, fc = ks - 1;
                        const f32x4 v = oa[1][fq][fc] * w1[fq] + oa[0][fq][fc];
                        *(LAS u32x2*)(pw + fq * 16 * PPITCH + fc * 32) = (u32x2){pack2(v.xy), pack2(v.zw)};
                    }
                    if (i < 32) SB();
                }
            }
            if (__builtin_expect(!__any(m == EMPTY), 1)) break;
            asm volatile("" ::: "memory");
        }
    }
    asm volatile("" :: "v"(pa), "v"(pb), "v"(pc_));
    wg_barrier();
    if (wave < 6) {
        u32x4 pv[4];
        LAS char* pr = lds + L_H + oq * PPITCH + oc * 16;
        UNROLL for (int k = 0; k < 4; ++k) pv[k] = U4(pr, k * PPITCH * 64);
        const f32x4 gt0 = F4(lds, L_MOD + 3072 + (48 * j + 8 * oc) * 4), gt1 = F4(lds, L_MOD + 3072 + (48 * j + 8 * oc) * 4 + 16);
        f32x2 o[4];
        UNROLL for (int e = 0; e < 4; ++e) o[e] = (unpack2(pv[0][e]) + unpack2(pv[1][e])) + (unpack2(pv[2][e]) + unpack2(pv[3][e]));
        u32x4 w = {pack2(o[0] * gt0.xy + unpack2(xr[0])), pack2(o[1] * gt0.zw + unpack2(xr[1])),
                   pack2(o[2] * gt1.xy + unpack2(xr[2])), pack2(o[3] * gt1.zw + unpack2(xr[3]))};
        { const __amdgpu_buffer_rsrc_t ro = __builtin_amdgcn_make_buffer_rsrc(out, 0, 2048 * 768, 0x00020000); __builtin_amdgcn_raw_buffer_store_b128(w, ro, ooff * 2, 0, 0); }
    }
    asm volatile(".Lpf_end:");
}

extern "C" void kernel_launch(void* const* d_in, const int* in_sizes, int n_in, void* d_out, int out_size, void* d_ws, size_t ws_size, hipStream_t stream) {
    dit_fused<<<256, 512, 0, stream>>>((const u16*)d_in[0], (const u16*)d_in[1], (const u16*)d_in[2], (const u16*)d_in[3], (const u16*)d_in[4], (u16*)d_out, (char*)d_ws);
}
```
